```python
import torch

def _softmax(x, axis=-1):
    return torch.softmax(x, dim=axis)
def _gelu(x):
    return torch.nn.functional.gelu(x, approximate="tanh")
def _silu(x):
    return torch.nn.functional.silu(x)
def _sigmoid(x):
    return torch.sigmoid(x)
def _layernorm(x, axis=-1, eps=1e-5):
    m = x.mean(dim=axis, keepdim=True)
    v = x.var(dim=axis, keepdim=True, unbiased=False)
    return (x - m) / torch.sqrt(v + eps)
def _scatter_add(tgt, idx, src, axis=0):
    return tgt.clone().index_add_(axis, idx, src)
def _segment_sum(src, seg, n):
    out = torch.zeros((n,) + src.shape[1:], dtype=src.dtype)
    return out.index_add_(0, seg, src)
def _pad(x, pw):
    flat = tuple(v for pair in reversed(pw) for v in pair)
    return torch.nn.functional.pad(x, flat)

kernel_name = 'sequential_preLN__spectral_fnet__moe_arch_mod__prompt'

def setup_inputs():

    _w = (lambda a: (lambda t: t.to(torch.bfloat16) if t.is_floating_point() else t)(torch.as_tensor(a)))
    def _A_setup():
        B, S, D = 1, 2048, 640
        ns = torch.arange(S)[:, None] * torch.arange(S)[None, :]
        nd = torch.arange(D)[:, None] * torch.arange(D)[None, :]
        Ws_re = torch.cos(-2.0 * torch.pi * ns / S).to(float)
        Ws_im = torch.sin(-2.0 * torch.pi * ns / S).to(float)
        Wd_re = torch.cos(-2.0 * torch.pi * nd / D).to(float)
        Wd_im = torch.sin(-2.0 * torch.pi * nd / D).to(float)
        return {
            "x": _w((lambda *s: torch.randn(*s, dtype=torch.bfloat16) * 0.1)(B, S, D)),
            "dft_seq_re": _w(Ws_re),
            "dft_seq_im": _w(Ws_im),
            "dft_hid_re": _w(Wd_re),
            "dft_hid_im": _w(Wd_im),
        }
    _A_in = _A_setup()

    def _B_setup():
        B, S, D, F, C = 1, 2048, 640, 1536, 8
        return {
            "x": _w((lambda *s: torch.randn(*s, dtype=torch.bfloat16) * 0.1)(B, S, D)),
            "w_r": _w((lambda *s: torch.randn(*s, dtype=torch.bfloat16) * 1.6)(D)),
            "w_gate": _w((lambda *s: torch.randn(*s, dtype=torch.bfloat16) * 0.8)(D, F)),
            "w_up": _w((lambda *s: torch.randn(*s, dtype=torch.bfloat16) * 0.8)(D, F)),
            "w_down": _w((lambda *s: torch.randn(*s, dtype=torch.bfloat16) * 0.8)(F, D)),
        }
    _B_in = _B_setup()

    return {
        "A_x": _A_in["x"],
        "A_dft_seq_re": _A_in["dft_seq_re"],
        "A_dft_seq_im": _A_in["dft_seq_im"],
        "A_dft_hid_re": _A_in["dft_hid_re"],
        "A_dft_hid_im": _A_in["dft_hid_im"],
        "B_x": _B_in["x"],
        "B_w_r": _B_in["w_r"],
        "B_w_gate": _B_in["w_gate"],
        "B_w_up": _B_in["w_up"],
        "B_w_down": _B_in["w_down"],
    }

def reference(A_x, A_dft_seq_re, A_dft_seq_im, A_dft_hid_re, A_dft_hid_im, B_x, B_w_r, B_w_gate, B_w_up, B_w_down):
    def _A_ref(x, dft_seq_re, dft_seq_im, dft_hid_re, dft_hid_im):
        a = torch.einsum("bsd,ed->bse", x, dft_hid_re)
        b = torch.einsum("bsd,ed->bse", x, dft_hid_im)
        y = torch.einsum("ts,bse->bte", dft_seq_re, a) - torch.einsum("ts,bse->bte", dft_seq_im, b)
        return y

    def _B_ref(x, w_r, w_gate, w_up, w_down):
        B, S, D = x.shape
        C = 8
        r = x @ w_r
        idx = torch.argsort(-r, axis=-1)[:, :C]
        idx_e = (lambda x, n, axis: torch.repeat_interleave(x, n, dim=axis))(idx.reshape(B, C, 1), D, axis=2)
        x_sel = (lambda x, i, axis: torch.take_along_dim(x, i, dim=axis))(x, idx_e, axis=1)
        g = _silu(x_sel @ w_gate)
        u = x_sel @ w_up
        y = (g * u) @ w_down
        r_sel = _sigmoid((lambda x, i, axis: torch.take_along_dim(x, i, dim=axis))(r, idx, axis=1))
        y = y * r_sel.reshape(B, C, 1)
        b_off = (torch.arange(B) * S).reshape(B, 1)
        flat_idx = (idx + b_off).reshape(B * C)
        zero = (x * 0.0).reshape(B * S, D)
        upd = _scatter_add(zero, flat_idx, y.reshape(B * C, D), axis=0)
        return x + upd.reshape(B, S, D)

    x = A_x
    h = x + _A_ref(_layernorm(x, axis=-1), A_dft_seq_re, A_dft_seq_im, A_dft_hid_re, A_dft_hid_im)
    h2 = h + _B_ref(_layernorm(h, axis=-1), B_w_r, B_w_gate, B_w_up, B_w_down)
    return h2
```

```cpp
#include <hip/hip_runtime.h>
#include <hip/hip_bf16.h>
#include <stdint.h>

#define SEQ 2048
#define DM 640
#define FF 1536
#define TOPC 8

typedef unsigned short u16;
typedef __attribute__((ext_vector_type(8))) short bf16x8;
typedef __attribute__((ext_vector_type(4))) short s16x4;
typedef __attribute__((ext_vector_type(4))) float f32x4;
typedef __attribute__((ext_vector_type(2))) float f32x2;
typedef __attribute__((ext_vector_type(2))) __bf16 bf16x2_t;
typedef __attribute__((ext_vector_type(4))) unsigned u32x4;
typedef __attribute__((ext_vector_type(2))) unsigned u32x2;

__device__ __forceinline__ unsigned pk2(float a, float b) {
  f32x2 v = {a, b};
  bf16x2_t r = __builtin_convertvector(v, bf16x2_t);
  return __builtin_bit_cast(unsigned, r);
}
__device__ __forceinline__ float lo2f(unsigned w) { return __uint_as_float(w << 16); }
__device__ __forceinline__ float hi2f(unsigned w) { return __uint_as_float(w & 0xffff0000u); }
__device__ __forceinline__ float rbf(float a) { return lo2f(pk2(a, 0.f)); }
__device__ __forceinline__ float bf2f(u16 u) { return __uint_as_float(((unsigned)u) << 16); }

#define WAIT_V(n) asm volatile("s_waitcnt vmcnt(%0)" ::"n"(n) : "memory")
#define WAIT_L(n) asm volatile("s_waitcnt lgkmcnt(%0)" ::"n"(n) : "memory")
#define RAW_BARRIER() do { WAIT_L(0); __builtin_amdgcn_s_barrier(); } while (0)

#define GLDS16(gptr, lptr) __builtin_amdgcn_global_load_lds((const unsigned*)(gptr), (unsigned*)(lptr), 16, 0, 0)

__device__ __forceinline__ unsigned wave_max_u32(unsigned v) {
  v = max(v, (unsigned)__builtin_amdgcn_update_dpp(0, (int)v, 0x111, 0xf, 0xf, false));
  v = max(v, (unsigned)__builtin_amdgcn_update_dpp(0, (int)v, 0x112, 0xf, 0xf, false));
  v = max(v, (unsigned)__builtin_amdgcn_update_dpp(0, (int)v, 0x114, 0xf, 0xf, false));
  v = max(v, (unsigned)__builtin_amdgcn_update_dpp(0, (int)v, 0x118, 0xf, 0xf, false));
  v = max(v, (unsigned)__builtin_amdgcn_update_dpp(0, (int)v, 0x142, 0xa, 0xf, false));
  v = max(v, (unsigned)__builtin_amdgcn_update_dpp(0, (int)v, 0x143, 0xc, 0xf, false));
  return (unsigned)__builtin_amdgcn_readlane((int)v, 63);
}
__device__ __forceinline__ float dpp_f(float v, int ctrl_dummy);
template <int CTRL, int RM>
__device__ __forceinline__ float dppf(float v) {
  return __int_as_float(__builtin_amdgcn_update_dpp(0, __float_as_int(v), CTRL, RM, 0xf, false));
}
__device__ __forceinline__ float wave_sum_f32(float v) {
  v += dppf<0x111, 0xf>(v);
  v += dppf<0x112, 0xf>(v);
  v += dppf<0x114, 0xf>(v);
  v += dppf<0x118, 0xf>(v);
  v += dppf<0x142, 0xa>(v);
  v += dppf<0x143, 0xc>(v);
  return __int_as_float(__builtin_amdgcn_readlane(__float_as_int(v), 63));
}

__global__ __launch_bounds__(256) void k1_ln_gemm(const u16* __restrict__ x, const u16* __restrict__ wre,
                                                   const u16* __restrict__ wim, u16* __restrict__ abT) {
  __shared__ __attribute__((aligned(1024))) char smem[10 * 10240];
  const int tid = threadIdx.x, w = tid >> 6, lane = tid & 63, q = lane >> 4, r = lane & 15;
  const int xcd = blockIdx.x & 7, j = blockIdx.x >> 3;
  const int mt = (xcd >> 1) * 4 + (j & 3);
  const int nt = (xcd & 1) * 8 + (j >> 2);
  const u16* W = (nt < 8) ? (wre + (size_t)nt * 80 * DM) : (wim + (size_t)(nt - 8) * 80 * DM);
  const int s0 = mt * 128;

  u32x4 a[2][20];
#pragma unroll
  for (int m = 0; m < 2; ++m) {
    const u16* xr = x + (size_t)(s0 + w * 32 + m * 16 + r) * DM + q * 8;
#pragma unroll
    for (int ks = 0; ks < 20; ++ks) a[m][ks] = *(const u32x4*)(xr + ks * 32);
  }
  {
    const int rin = lane >> 3, pos = lane & 7;
#pragma unroll
    for (int i = 0; i < 25; ++i) {
      const int pc = w * 25 + i;
      const int kb = pc / 10, p = pc % 10;
      const int row = p * 8 + rin;
      const int chunk = pos ^ ((row >> 1) & 7);
      GLDS16(W + (size_t)row * DM + kb * 64 + chunk * 8, smem + kb * 10240 + p * 1024);
    }
  }
#pragma unroll
  for (int m = 0; m < 2; ++m) {
    float s = 0.f;
#pragma unroll
    for (int ks = 0; ks < 20; ++ks)
#pragma unroll
      for (int c = 0; c < 4; ++c) s += lo2f(a[m][ks][c]) + hi2f(a[m][ks][c]);
    s += __shfl_xor(s, 16);
    s += __shfl_xor(s, 32);
    const float mean = s * (1.0f / DM);
    const float mb = rbf(mean);
    float ss = 0.f;
#pragma unroll
    for (int ks = 0; ks < 20; ++ks)
#pragma unroll
      for (int c = 0; c < 4; ++c) {
        const float d0 = lo2f(a[m][ks][c]) - mb, d1 = hi2f(a[m][ks][c]) - mb;
        ss += d0 * d0;
        ss += d1 * d1;
        a[m][ks][c] = pk2(d0, d1);
      }
    ss += __shfl_xor(ss, 16);
    ss += __shfl_xor(ss, 32);
    const float dm = mean - mb;
    float var = ss * (1.0f / DM) - dm * dm;
    var = fmaxf(var, 0.f);
    const float vb = rbf(var);
    const float sd = rbf(sqrtf(rbf(vb + 1e-5f)));
    const float rs = 1.0f / sd;
#pragma unroll
    for (int ks = 0; ks < 20; ++ks)
#pragma unroll
      for (int c = 0; c < 4; ++c) a[m][ks][c] = pk2(lo2f(a[m][ks][c]) * rs, hi2f(a[m][ks][c]) * rs);
  }
  WAIT_V(0);
  __builtin_amdgcn_s_barrier();

  f32x4 acc[2][5];
#pragma unroll
  for (int m = 0; m < 2; ++m)
#pragma unroll
    for (int n = 0; n < 5; ++n) acc[m][n] = (f32x4){0.f, 0.f, 0.f, 0.f};
  const int sw = (r >> 1) & 7;
#pragma unroll
  for (int ks = 0; ks < 20; ++ks) {
    const int kb = ks >> 1, chunk = (ks & 1) * 4 + q;
    bf16x8 b[5];
#pragma unroll
    for (int n = 0; n < 5; ++n)
      b[n] = *(const bf16x8*)(smem + kb * 10240 + (n * 16 + r) * 128 + ((chunk ^ sw) << 4));
#pragma unroll
    for (int m = 0; m < 2; ++m)
#pragma unroll
      for (int n = 0; n < 5; ++n)
        acc[m][n] = __builtin_amdgcn_mfma_f32_16x16x32_bf16(__builtin_bit_cast(bf16x8, a[m][ks]), b[n], acc[m][n], 0, 0, 0);
  }
#pragma unroll
  for (int m = 0; m < 2; ++m)
#pragma unroll
    for (int n = 0; n < 5; ++n) {
      u32x2 o;
      o[0] = pk2(acc[m][n][0], acc[m][n][1]);
      o[1] = pk2(acc[m][n][2], acc[m][n][3]);
      *(u32x2*)(abT + (size_t)(nt * 80 + n * 16 + r) * SEQ + s0 + w * 32 + m * 16 + q * 4) = o;
    }
}

#define K2_STAGE 53248
__global__ __launch_bounds__(256) void k2_gemm(const u16* __restrict__ wsre, const u16* __restrict__ wsim,
                                                const u16* __restrict__ abT, u16* __restrict__ ypart) {
  __shared__ __attribute__((aligned(1024))) char smem[3 * K2_STAGE];
  const int tid = threadIdx.x, w = tid >> 6, lane = tid & 63, q = lane >> 4, r = lane & 15;
  const int xcd = blockIdx.x & 7, j = blockIdx.x >> 3;
  const int part = xcd >> 2;
  const int mt = (xcd & 3) * 4 + (j & 3);
  const int nt = j >> 2;
  const int t0 = mt * 128, e0 = nt * 80;
  const u16* A = (part ? wsim : wsre) + (size_t)t0 * SEQ;
  const u16* B = abT + (size_t)(part * DM + e0) * SEQ;

  const int rin = lane >> 3, pos = lane & 7;
  int aoff[4], boff[5], bl[5];
#pragma unroll
  for (int p = 0; p < 4; ++p) {
    const int row = w * 32 + p * 8 + rin;
    aoff[p] = row * SEQ + ((pos ^ ((row >> 1) & 7)) << 3);
  }
#pragma unroll
  for (int i = 0; i < 5; ++i) {
    const int pc = w * 5 + i;
    const int sub = pc / 10, p = pc % 10;
    const int row = p * 8 + rin;
    boff[i] = row * SEQ + sub * 64 + ((pos ^ ((row >> 1) & 7)) << 3);
    bl[i] = 32768 + sub * 10240 + p * 1024;
  }
#define K2_ISSUE(stage, buf)                                                          \
  do {                                                                                \
    char* sb_ = smem + (buf) * K2_STAGE;                                              \
    const int kb_ = (stage) * 128;                                                    \
    _Pragma("unroll") for (int sub = 0; sub < 2; ++sub)                               \
      _Pragma("unroll") for (int p = 0; p < 4; ++p)                                   \
        GLDS16(A + aoff[p] + kb_ + sub * 64, sb_ + sub * 16384 + (w * 4 + p) * 1024); \
    _Pragma("unroll") for (int i = 0; i < 5; ++i)                                     \
      GLDS16(B + boff[i] + kb_, sb_ + bl[i]);                                         \
  } while (0)

  f32x4 acc[2][5];
#pragma unroll
  for (int m = 0; m < 2; ++m)
#pragma unroll
    for (int n = 0; n < 5; ++n) acc[m][n] = (f32x4){0.f, 0.f, 0.f, 0.f};
  const int sw = (r >> 1) & 7;

  K2_ISSUE(0, 0);
  K2_ISSUE(1, 1);
  int buf = 0;
  for (int st = 0; st < 16; ++st) {
    if (st < 15) WAIT_V(13); else WAIT_V(0);
    RAW_BARRIER();
    if (st + 2 < 16) {
      int nb = buf + 2; if (nb >= 3) nb -= 3;
      K2_ISSUE(st + 2, nb);
    }
    const char* sb = smem + buf * K2_STAGE;
#pragma unroll
    for (int sub = 0; sub < 2; ++sub)
#pragma unroll
      for (int h = 0; h < 2; ++h) {
        const int co = ((h * 4 + q) ^ sw) << 4;
        bf16x8 af[2], bfr[5];
#pragma unroll
        for (int m = 0; m < 2; ++m) af[m] = *(const bf16x8*)(sb + sub * 16384 + (w * 32 + m * 16 + r) * 128 + co);
#pragma unroll
        for (int n = 0; n < 5; ++n) bfr[n] = *(const bf16x8*)(sb + 32768 + sub * 10240 + (n * 16 + r) * 128 + co);
#pragma unroll
        for (int m = 0; m < 2; ++m)
#pragma unroll
          for (int n = 0; n < 5; ++n)
            acc[m][n] = __builtin_amdgcn_mfma_f32_16x16x32_bf16(bfr[n], af[m], acc[m][n], 0, 0, 0);
      }
    buf = buf + 1; if (buf >= 3) buf = 0;
  }
  u16* Y = ypart + (size_t)part * SEQ * DM;
#pragma unroll
  for (int m = 0; m < 2; ++m)
#pragma unroll
    for (int n = 0; n < 5; ++n) {
      u32x2 o;
      o[0] = pk2(acc[m][n][0], acc[m][n][1]);
      o[1] = pk2(acc[m][n][2], acc[m][n][3]);
      *(u32x2*)(Y + (size_t)(t0 + w * 32 + m * 16 + r) * DM + e0 + n * 16 + q * 4) = o;
    }
}

__global__ __launch_bounds__(512) void k3_ln2(const u16* __restrict__ x, const u16* __restrict__ ypart,
                                               const u16* __restrict__ wr, u16* __restrict__ out,
                                               float* __restrict__ rsc, float2* __restrict__ stats) {
  const int lane = threadIdx.x & 63;
  const int row = blockIdx.x * 8 + (threadIdx.x >> 6);
  const unsigned* xp = (const unsigned*)(x + (size_t)row * DM);
  const unsigned* yr = (const unsigned*)(ypart + (size_t)row * DM);
  const unsigned* yi = (const unsigned*)(ypart + (size_t)SEQ * DM + (size_t)row * DM);
  const unsigned* wp = (const unsigned*)wr;
  unsigned xv[5], rv[5], iv[5], wv[5];
#pragma unroll
  for (int i = 0; i < 5; ++i) {
    xv[i] = xp[i * 64 + lane];
    rv[i] = yr[i * 64 + lane];
    iv[i] = yi[i * 64 + lane];
    wv[i] = wp[i * 64 + lane];
  }
  float h[10];
  float s = 0.f;
#pragma unroll
  for (int i = 0; i < 5; ++i) {
    const float y0 = rbf(lo2f(rv[i]) - lo2f(iv[i])), y1 = rbf(hi2f(rv[i]) - hi2f(iv[i]));
    h[2 * i] = rbf(lo2f(xv[i]) + y0);
    h[2 * i + 1] = rbf(hi2f(xv[i]) + y1);
    s += h[2 * i] + h[2 * i + 1];
  }
  s = wave_sum_f32(s);
  const float mean = s * (1.0f / DM);
  const float mb = rbf(mean);
  float d[10];
  float ss = 0.f;
#pragma unroll
  for (int i = 0; i < 10; ++i) {
    d[i] = h[i] - mb;
    ss += d[i] * d[i];
  }
  ss = wave_sum_f32(ss);
  const float dm = mean - mb;
  float var = fmaxf(ss * (1.0f / DM) - dm * dm, 0.f);
  const float vb = rbf(var);
  const float sd = rbf(sqrtf(rbf(vb + 1e-5f)));
  const float rs = 1.0f / sd;
  float dot = 0.f;
  unsigned* op = (unsigned*)(out + (size_t)row * DM);
#pragma unroll
  for (int i = 0; i < 5; ++i) {
    const float n0 = rbf(rbf(d[2 * i]) * rs), n1 = rbf(rbf(d[2 * i + 1]) * rs);
    dot += n0 * lo2f(wv[i]);
    dot += n1 * hi2f(wv[i]);
    op[i * 64 + lane] = pk2(h[2 * i] + n0, h[2 * i + 1] + n1);
  }
  dot = wave_sum_f32(dot);
  if (lane == 0) {
    rsc[row] = rbf(dot);
    stats[row] = make_float2(mb, rs);
  }
}

__device__ __forceinline__ void block_top8(const float* __restrict__ rsc, unsigned* cand  , int* sel  ) {
  const int tid = threadIdx.x, w = tid >> 6, lane = tid & 63;
  unsigned key[8];
  {
    const f32x4 v0 = *(const f32x4*)(rsc + tid * 8);
    const f32x4 v1 = *(const f32x4*)(rsc + tid * 8 + 4);
#pragma unroll
    for (int i = 0; i < 8; ++i) {
      const unsigned u = __float_as_uint(i < 4 ? v0[i] : v1[i - 4]);
      const unsigned o = (u & 0x80000000u) ? ~u : (u | 0x80000000u);
      key[i] = (o & 0xffff0000u) | (0xffffu - (unsigned)(tid * 8 + i));
    }
  }
#pragma unroll
  for (int rnd = 0; rnd < 8; ++rnd) {
    unsigned lm = key[0];
#pragma unroll
    for (int i = 1; i < 8; ++i) lm = max(lm, key[i]);
    const unsigned wm = wave_max_u32(lm);
#pragma unroll
    for (int i = 0; i < 8; ++i) key[i] = (key[i] == wm) ? 0u : key[i];
    if (lane == 0) cand[w * 8 + rnd] = wm;
  }
  __syncthreads();
  if (w == 0) {
    unsigned k2 = (lane < 32) ? cand[lane] : 0u;
#pragma unroll
    for (int rnd = 0; rnd < 8; ++rnd) {
      const unsigned wm = wave_max_u32(k2);
      k2 = (k2 == wm) ? 0u : k2;
      if (lane == 0) sel[rnd] = (int)(0xffffu - (wm & 0xffffu));
    }
  }
  __syncthreads();
}

__device__ __forceinline__ bf16x8 ldtr_frag(const char* tile, int k0, int lane) {
  const int g = lane >> 4, i = lane & 15;
  const char* p = tile + (k0 + 8 * g + (i >> 2)) * 32 + (i & 3) * 8;
  s16x4 lo = __builtin_amdgcn_ds_read_tr16_b64_v4i16((__attribute__((address_space(3))) s16x4*)(p));
  s16x4 hi = __builtin_amdgcn_ds_read_tr16_b64_v4i16((__attribute__((address_space(3))) s16x4*)(p + 128));
  bf16x8 f;
  f[0] = lo[0]; f[1] = lo[1]; f[2] = lo[2]; f[3] = lo[3];
  f[4] = hi[0]; f[5] = hi[1]; f[6] = hi[2]; f[7] = hi[3];
  return f;
}

#define XS_LD 1296
__global__ __launch_bounds__(256) void k4_gateup(const u16* __restrict__ x, const u16* __restrict__ ypart,
                                                  const float* __restrict__ rsc, const float2* __restrict__ stats,
                                                  const u16* __restrict__ wg, const u16* __restrict__ wu,
                                                  u16* __restrict__ act, int* __restrict__ selg) {
  __shared__ __attribute__((aligned(1024))) char smem[20480 * 2 + 16 * XS_LD + 8192 + 128 + 32];
  char* tg = smem;
  char* tu = smem + 20480;
  char* xs = smem + 40960;
  float* red = (float*)(smem + 40960 + 16 * XS_LD);
  unsigned* cand = (unsigned*)(smem + 40960 + 16 * XS_LD + 8192);
  int* sel = (int*)(smem + 40960 + 16 * XS_LD + 8192 + 128);
  const int tid = threadIdx.x, w = tid >> 6, lane = tid & 63, q = lane >> 4, r = lane & 15;
  const int xcd = blockIdx.x & 7, jj = blockIdx.x >> 3;
  const int cg = (jj >> 2) * 32 + xcd * 4 + (jj & 3);
  const int f0 = cg * 16;
#pragma unroll
  for (int i = 0; i < 5; ++i) {
    const int p = w * 5 + i;
    const int row = p * 32 + (lane >> 1);
    GLDS16(wg + (size_t)row * FF + f0 + (lane & 1) * 8, tg + p * 1024);
    GLDS16(wu + (size_t)row * FF + f0 + (lane & 1) * 8, tu + p * 1024);
  }
  block_top8(rsc, cand, sel);
  {
    const int tk = tid >> 5, l32 = tid & 31;
    const int row = sel[tk];
    const float2 st = stats[row];
    const unsigned* xp = (const unsigned*)(x + (size_t)row * DM);
    const unsigned* yr = (const unsigned*)(ypart + (size_t)row * DM);
    const unsigned* yi = (const unsigned*)(ypart + (size_t)SEQ * DM + (size_t)row * DM);
#pragma unroll
    for (int i = 0; i < 10; ++i) {
      const int c = i * 32 + l32;
      const unsigned xv = xp[c], rv = yr[c], iv = yi[c];
      const float h0 = rbf(lo2f(xv) + rbf(lo2f(rv) - lo2f(iv)));
      const float h1 = rbf(hi2f(xv) + rbf(hi2f(rv) - hi2f(iv)));
      *(unsigned*)(xs + tk * XS_LD + c * 4) = pk2(rbf(h0 - st.x) * st.y, rbf(h1 - st.x) * st.y);
      *(unsigned*)(xs + (tk + 8) * XS_LD + c * 4) = 0u;
    }
    if (blockIdx.x == 0 && tid < 8) selg[tid] = sel[tid];
  }
  WAIT_V(0);
  __syncthreads();
  f32x4 ag = {0.f, 0.f, 0.f, 0.f}, au = {0.f, 0.f, 0.f, 0.f};
#pragma unroll
  for (int ks = 0; ks < 5; ++ks) {
    const int k0 = w * 160 + ks * 32;
    const bf16x8 af = *(const bf16x8*)(xs + r * XS_LD + (k0 + q * 8) * 2);
    const bf16x8 bg = ldtr_frag(tg, k0, lane);
    const bf16x8 bu = ldtr_frag(tu, k0, lane);
    ag = __builtin_amdgcn_mfma_f32_16x16x32_bf16(af, bg, ag, 0, 0, 0);
    au = __builtin_amdgcn_mfma_f32_16x16x32_bf16(af, bu, au, 0, 0, 0);
  }
  *(f32x4*)(red + (w * 2 + 0) * 256 + lane * 4) = ag;
  *(f32x4*)(red + (w * 2 + 1) * 256 + lane * 4) = au;
  __syncthreads();
  if (tid < 128) {
    const int tk = tid >> 4, c = tid & 15;
    const int li = ((tk >> 2) * 16 + c) * 4 + (tk & 3);
    float g = 0.f, u = 0.f;
#pragma unroll
    for (int ww = 0; ww < 4; ++ww) {
      g += red[(ww * 2 + 0) * 256 + li];
      u += red[(ww * 2 + 1) * 256 + li];
    }
    g = rbf(g);
    u = rbf(u);
    const float sg = rbf(g / (1.0f + __expf(-g)));
    const unsigned o = pk2(sg * u, 0.f);
    act[tk * FF + f0 + c] = (u16)(o & 0xffffu);
  }
}

#define AC_LD 3088
__global__ __launch_bounds__(256) void k5_down(const u16* __restrict__ x, const u16* __restrict__ ypart,
                                                const float* __restrict__ rsc, const float2* __restrict__ stats,
                                                const u16* __restrict__ wd, const u16* __restrict__ act,
                                                const int* __restrict__ selg, u16* __restrict__ out) {
  __shared__ __attribute__((aligned(1024))) char smem[49152 + 16 * AC_LD + 4096];
  char* td = smem;
  char* as = smem + 49152;
  float* red = (float*)(smem + 49152 + 16 * AC_LD);
  const int tid = threadIdx.x, w = tid >> 6, lane = tid & 63, q = lane >> 4, r = lane & 15;
  const int xcd = blockIdx.x & 7, jj = blockIdx.x >> 3;
  const int cg = xcd * 5 + jj;
  const int c0 = cg * 16;
#pragma unroll
  for (int i = 0; i < 12; ++i) {
    const int p = w * 12 + i;
    const int row = p * 32 + (lane >> 1);
    GLDS16(wd + (size_t)row * DM + c0 + (lane & 1) * 8, td + p * 1024);
  }
#pragma unroll
  for (int i = 0; i < 6; ++i) {
    const int idx = i * 256 + tid;
    const int tk = idx / 192, ch = idx % 192;
    *(u32x4*)(as + tk * AC_LD + ch * 16) = *(const u32x4*)(act + tk * FF + ch * 8);
    *(u32x4*)(as + (tk + 8) * AC_LD + ch * 16) = (u32x4){0u, 0u, 0u, 0u};
  }
  WAIT_V(0);
  __syncthreads();
  f32x4 acc = {0.f, 0.f, 0.f, 0.f};
#pragma unroll
  for (int ks = 0; ks < 12; ++ks) {
    const int k0 = w * 384 + ks * 32;
    const bf16x8 af = *(const bf16x8*)(as + r * AC_LD + (k0 + q * 8) * 2);
    const bf16x8 bd = ldtr_frag(td, k0, lane);
    acc = __builtin_amdgcn_mfma_f32_16x16x32_bf16(af, bd, acc, 0, 0, 0);
  }
  *(f32x4*)(red + w * 256 + lane * 4) = acc;
  __syncthreads();
  if (tid < 128) {
    const int tk = tid >> 4, c = tid & 15;
    const int li = ((tk >> 2) * 16 + c) * 4 + (tk & 3);
    float y = 0.f;
#pragma unroll
    for (int ww = 0; ww < 4; ++ww) y += red[ww * 256 + li];
    y = rbf(y);
    const int row = selg[tk];
    const float rr = rsc[row];
    const float sgm = rbf(1.0f / (1.0f + __expf(-rr)));
    y = rbf(y * sgm);
    const float2 st = stats[row];
    const int col = c0 + c;
    const float xv = bf2f(x[(size_t)row * DM + col]);
    const float yr = bf2f(ypart[(size_t)row * DM + col]);
    const float yi = bf2f(ypart[(size_t)SEQ * DM + (size_t)row * DM + col]);
    const float h = rbf(xv + rbf(yr - yi));
    const float hn = rbf(rbf(h - st.x) * st.y);
    const float bo = rbf(hn + y);
    const unsigned o = pk2(h + bo, 0.f);
    out[(size_t)row * DM + col] = (u16)(o & 0xffffu);
  }
}

extern "C" void kernel_launch(void* const* d_in, const int* in_sizes, int n_in, void* d_out, int out_size,
                              void* d_ws, size_t ws_size, hipStream_t stream) {
  const u16* x = (const u16*)d_in[0];
  const u16* wsre = (const u16*)d_in[1];
  const u16* wsim = (const u16*)d_in[2];
  const u16* wdre = (const u16*)d_in[3];
  const u16* wdim = (const u16*)d_in[4];
  const u16* wr = (const u16*)d_in[6];
  const u16* wg = (const u16*)d_in[7];
  const u16* wu = (const u16*)d_in[8];
  const u16* wdn = (const u16*)d_in[9];
  u16* out = (u16*)d_out;
  char* ws = (char*)d_ws;
  u16* abT = (u16*)ws;
  u16* ypart = (u16*)(ws + 5242880);
  float* rsc = (float*)(ws + 10485760);
  float2* stats = (float2*)(ws + 10485760 + 8192);
  u16* act = (u16*)(ws + 10485760 + 8192 + 16384);
  int* selg = (int*)(ws + 10485760 + 8192 + 16384 + 24576);

  k1_ln_gemm<<<256, 256, 0, stream>>>(x, wdre, wdim, abT);
  k2_gemm<<<256, 256, 0, stream>>>(wsre, wsim, abT, ypart);
  k3_ln2<<<256, 512, 0, stream>>>(x, ypart, wr, out, rsc, stats);
  k4_gateup<<<96, 256, 0, stream>>>(x, ypart, rsc, stats, wg, wu, act, selg);
  k5_down<<<40, 256, 0, stream>>>(x, ypart, rsc, stats, wdn, act, selg, out);
}
```

```cpp
#include <hip/hip_runtime.h>
#include <hip/hip_bf16.h>
#include <stdint.h>

#define K1_PREFETCH 1
#define SEQ 2048
#define DM 640
#define FF 1536
#define TOPC 8

typedef unsigned short u16;
typedef __attribute__((ext_vector_type(8))) short bf16x8;
typedef __attribute__((ext_vector_type(4))) short s16x4;
typedef __attribute__((ext_vector_type(4))) float f32x4;
typedef __attribute__((ext_vector_type(2))) float f32x2;
typedef __attribute__((ext_vector_type(2))) __bf16 bf16x2_t;
typedef __attribute__((ext_vector_type(4))) unsigned u32x4;
typedef __attribute__((ext_vector_type(2))) unsigned u32x2;

__device__ __forceinline__ unsigned pk2(float a, float b) {
  f32x2 v = {a, b};
  bf16x2_t r = __builtin_convertvector(v, bf16x2_t);
  return __builtin_bit_cast(unsigned, r);
}
__device__ __forceinline__ float lo2f(unsigned w) { return __uint_as_float(w << 16); }
__device__ __forceinline__ float hi2f(unsigned w) { return __uint_as_float(w & 0xffff0000u); }
__device__ __forceinline__ float rbf(float a) { return lo2f(pk2(a, 0.f)); }
__device__ __forceinline__ float bf2f(u16 u) { return __uint_as_float(((unsigned)u) << 16); }

#define WAIT_V(n) asm volatile("s_waitcnt vmcnt(%0)" ::"n"(n) : "memory")
#define WAIT_L(n) asm volatile("s_waitcnt lgkmcnt(%0)" ::"n"(n) : "memory")
#define RAW_BARRIER() do { WAIT_L(0); __builtin_amdgcn_s_barrier(); } while (0)

#define GLDS16(gptr, lptr) __builtin_amdgcn_global_load_lds((const unsigned*)(gptr), (unsigned*)(lptr), 16, 0, 0)

__device__ __forceinline__ unsigned wave_max_u32(unsigned v) {
  v = max(v, (unsigned)__builtin_amdgcn_update_dpp(0, (int)v, 0x111, 0xf, 0xf, false));
  v = max(v, (unsigned)__builtin_amdgcn_update_dpp(0, (int)v, 0x112, 0xf, 0xf, false));
  v = max(v, (unsigned)__builtin_amdgcn_update_dpp(0, (int)v, 0x114, 0xf, 0xf, false));
  v = max(v, (unsigned)__builtin_amdgcn_update_dpp(0, (int)v, 0x118, 0xf, 0xf, false));
  v = max(v, (unsigned)__builtin_amdgcn_update_dpp(0, (int)v, 0x142, 0xa, 0xf, false));
  v = max(v, (unsigned)__builtin_amdgcn_update_dpp(0, (int)v, 0x143, 0xc, 0xf, false));
  return (unsigned)__builtin_amdgcn_readlane((int)v, 63);
}
__device__ __forceinline__ float dpp_f(float v, int ctrl_dummy);
template <int CTRL, int RM>
__device__ __forceinline__ float dppf(float v) {
  return __int_as_float(__builtin_amdgcn_update_dpp(0, __float_as_int(v), CTRL, RM, 0xf, false));
}
__device__ __forceinline__ float wave_sum_f32(float v) {
  v += dppf<0x111, 0xf>(v);
  v += dppf<0x112, 0xf>(v);
  v += dppf<0x114, 0xf>(v);
  v += dppf<0x118, 0xf>(v);
  v += dppf<0x142, 0xa>(v);
  v += dppf<0x143, 0xc>(v);
  return __int_as_float(__builtin_amdgcn_readlane(__float_as_int(v), 63));
}

#define K1_AOFF 0
#define K1_BOFF 81920
#define K1_BST 20480
#define K1_DUMMY (81920 + 3 * 20480)
__device__ __forceinline__ float red8(float v) {
  v += dppf<0xB1, 0xf>(v);
  v += dppf<0x4E, 0xf>(v);
  v += dppf<0x141, 0xf>(v);
  return v;
}
__global__ __launch_bounds__(576) void k1_ln_gemm(const u16* __restrict__ x, const u16* __restrict__ wre,
                                                   const u16* __restrict__ wim, u16* __restrict__ abT,
                                                   const u16* __restrict__ wsre, const u16* __restrict__ wsim,
                                                   const u16* __restrict__ wg, const u16* __restrict__ wu,
                                                   const u16* __restrict__ wdn) {
  __shared__ __attribute__((aligned(1024))) char smem[K1_DUMMY + 1024];
  const int tid = threadIdx.x, lane = tid & 63, q = lane >> 4, r = lane & 15;
  const int w = __builtin_amdgcn_readfirstlane(tid >> 6);
  const int xcd = blockIdx.x & 7, j = blockIdx.x >> 3;
  const int mt = (xcd >> 1) * 8 + (j & 7);
  const int nt = (xcd & 1) * 4 + (j >> 3);
  const int s0 = mt * 64;

  if (w == 8) {
    const char* p = (const char*)((xcd >> 2) ? wsim : wsre) + (size_t)(xcd & 3) * 512 * SEQ * 2 + (size_t)j * 65536 + lane * 16;
    char* dl = smem + K1_DUMMY;
#pragma unroll 8
    for (int i = 0; i < 64; ++i) GLDS16(p + i * 1024, dl);
    const size_t fo = (size_t)blockIdx.x * 7680 + lane * 16;
#pragma unroll
    for (int i = 0; i < 7; ++i) {
      GLDS16((const char*)wg + fo + i * 1024, dl);
      GLDS16((const char*)wu + fo + i * 1024, dl);
      GLDS16((const char*)wdn + fo + i * 1024, dl);
    }
    if (lane < 32) {
      GLDS16((const char*)wg + fo + 7168, dl);
      GLDS16((const char*)wu + fo + 7168, dl);
      GLDS16((const char*)wdn + fo + 7168, dl);
    }
    WAIT_V(0);
    return;
  }

  const int row8 = lane >> 3, c8 = lane & 7;
  const u16* W = (nt < 4) ? (wre + (size_t)nt * 160 * DM) : (wim + (size_t)(nt - 4) * 160 * DM);
  u32x4 a[10];
  {
    const u16* xr = x + (size_t)(s0 + w * 8 + row8) * DM + c8 * 8;
#pragma unroll
    for (int jj = 0; jj < 10; ++jj) a[jj] = *(const u32x4*)(xr + jj * 64);
  }
  int boff[5];
  const int lw = w - 4;
  if (w >= 4) {
#pragma unroll
    for (int i = 0; i < 5; ++i) {
      const int row = (lw * 5 + i) * 8 + row8;
      boff[i] = row * DM + ((c8 ^ ((row >> 1) & 7)) << 3);
    }
#define K1_ISSUE(stage, buf)                                                                        \
  do {                                                                                              \
    _Pragma("unroll") for (int i = 0; i < 5; ++i)                                                   \
      GLDS16(W + boff[i] + (stage) * 64, smem + K1_BOFF + (buf) * K1_BST + (lw * 5 + i) * 1024);    \
  } while (0)
    K1_ISSUE(0, 0);
    K1_ISSUE(1, 1);
    K1_ISSUE(2, 2);
  }
  {
    float s = 0.f;
#pragma unroll
    for (int jj = 0; jj < 10; ++jj)
#pragma unroll
      for (int c = 0; c < 4; ++c) s += lo2f(a[jj][c]) + hi2f(a[jj][c]);
    s = red8(s);
    const float mean = s * (1.0f / DM);
    const float mb = rbf(mean);
    float ss = 0.f;
#pragma unroll
    for (int jj = 0; jj < 10; ++jj)
#pragma unroll
      for (int c = 0; c < 4; ++c) {
        const float d0 = lo2f(a[jj][c]) - mb, d1 = hi2f(a[jj][c]) - mb;
        ss += d0 * d0;
        ss += d1 * d1;
        a[jj][c] = pk2(d0, d1);
      }
    ss = red8(ss);
    const float dm = mean - mb;
    const float var = fmaxf(ss * (1.0f / DM) - dm * dm, 0.f);
    const float vb = rbf(var);
    const float sd = rbf(sqrtf(rbf(vb + 1e-5f)));
    const float rs = 1.0f / sd;
    const int row = w * 8 + row8;
    char* ap = smem + K1_AOFF + row * 128 + ((c8 ^ ((row >> 1) & 7)) << 4);
#pragma unroll
    for (int jj = 0; jj < 10; ++jj) {
      u32x4 o;
#pragma unroll
      for (int c = 0; c < 4; ++c) o[c] = pk2(lo2f(a[jj][c]) * rs, hi2f(a[jj][c]) * rs);
      *(u32x4*)(ap + jj * 8192) = o;
    }
  }
  if (w >= 4) {
    WAIT_V(10);
    WAIT_L(0);
    __builtin_amdgcn_s_barrier();
#pragma unroll
    for (int st = 0; st < 10; ++st) {
      if (st < 8) WAIT_V(5); else WAIT_V(0);
      __builtin_amdgcn_s_barrier();
      if (st + 3 < 10) K1_ISSUE(st + 3, st % 3);
    }
  } else {
    const int wm = w >> 1, wn = w & 1;
    f32x4 acc[2][5];
#pragma unroll
    for (int m = 0; m < 2; ++m)
#pragma unroll
      for (int n = 0; n < 5; ++n) acc[m][n] = (f32x4){0.f, 0.f, 0.f, 0.f};
    const int sw = (r >> 1) & 7;
    const int aro = K1_AOFF + (wm * 32 + r) * 128;
    const int bro = K1_BOFF + (wn * 80 + r) * 128;
    const int co0 = ((0 * 4 + q) ^ sw) << 4, co1 = ((1 * 4 + q) ^ sw) << 4;
    bf16x8 xa[2], xb[5], ya[2], yb[5];
#define K1_READ(fa, fb, st, h)                                                                      \
  do {                                                                                              \
    const int co_ = (h) ? co1 : co0;                                                                \
    _Pragma("unroll") for (int n = 0; n < 5; ++n)                                                   \
      fb[n] = *(const bf16x8*)(smem + bro + ((st) % 3) * K1_BST + n * 2048 + co_);                  \
    _Pragma("unroll") for (int m = 0; m < 2; ++m)                                                   \
      fa[m] = *(const bf16x8*)(smem + aro + (st) * 8192 + m * 2048 + co_);                          \
  } while (0)
#define K1_MMA(fa, fb)                                                                              \
  do {                                                                                              \
    _Pragma("unroll") for (int m = 0; m < 2; ++m)                                                   \
      _Pragma("unroll") for (int n = 0; n < 5; ++n)                                                 \
        acc[m][n] = __builtin_amdgcn_mfma_f32_16x16x32_bf16(fa[m], fb[n], acc[m][n], 0, 0, 0);      \
  } while (0)
#define SB() __builtin_amdgcn_sched_barrier(0)
    WAIT_L(0);
    __builtin_amdgcn_s_barrier();
    K1_READ(xa, xb, 0, 0);
#pragma unroll
    for (int st = 0; st < 10; ++st) {
      SB(); K1_READ(ya, yb, st, 1); SB(); K1_MMA(xa, xb);
      SB(); WAIT_L(0); __builtin_amdgcn_s_barrier(); SB();
      if (st < 9) K1_READ(xa, xb, st + 1, 0);
      SB(); K1_MMA(ya, yb);
    }
#pragma unroll
    for (int m = 0; m < 2; ++m)
#pragma unroll
      for (int n = 0; n < 5; ++n) {
        u32x2 o;
        o[0] = pk2(acc[m][n][0], acc[m][n][1]);
        o[1] = pk2(acc[m][n][2], acc[m][n][3]);
        *(u32x2*)(abT + (size_t)(nt * 160 + wn * 80 + n * 16 + r) * SEQ + s0 + wm * 32 + m * 16 + q * 4) = o;
      }
  }
}

#define K2_STAGE 53248
__global__ __launch_bounds__(512) void k2_gemm(const u16* __restrict__ wsre, const u16* __restrict__ wsim,
                                                const u16* __restrict__ abT, u16* __restrict__ ypart) {
  __shared__ __attribute__((aligned(1024))) char smem[3 * K2_STAGE];
  const int tid = threadIdx.x, lane = tid & 63, q = lane >> 4, r = lane & 15;
  const int w = __builtin_amdgcn_readfirstlane(tid >> 6);
  const int xcd = blockIdx.x & 7, j = blockIdx.x >> 3;
  const int part = xcd >> 2;
  const int mt = (xcd & 3) * 4 + (j & 3);
  const int nt = j >> 2;
  const int t0 = mt * 128, e0 = nt * 80;

  if (w >= 4) {
    const int lw = w - 4;
    const u16* A = (part ? wsim : wsre) + (size_t)t0 * SEQ;
    const u16* B = abT + (size_t)(part * DM + e0) * SEQ;
    const int rin = lane >> 3, pos = lane & 7;
    int aoff[4], boff[5], bl[5];
#pragma unroll
    for (int p = 0; p < 4; ++p) {
      const int row = lw * 32 + p * 8 + rin;
      aoff[p] = row * SEQ + ((pos ^ ((row >> 1) & 7)) << 3);
    }
#pragma unroll
    for (int i = 0; i < 5; ++i) {
      const int pc = lw * 5 + i;
      const int sub = pc / 10, p = pc % 10;
      const int row = p * 8 + rin;
      boff[i] = row * SEQ + sub * 64 + ((pos ^ ((row >> 1) & 7)) << 3);
      bl[i] = 32768 + sub * 10240 + p * 1024;
    }
#define K2_ISSUE(stage, buf)                                                           \
  do {                                                                                 \
    char* sb_ = smem + (buf) * K2_STAGE;                                               \
    const int kb_ = (stage) * 128;                                                     \
    _Pragma("unroll") for (int sub = 0; sub < 2; ++sub)                                \
      _Pragma("unroll") for (int p = 0; p < 4; ++p)                                    \
        GLDS16(A + aoff[p] + kb_ + sub * 64, sb_ + sub * 16384 + (lw * 4 + p) * 1024); \
    _Pragma("unroll") for (int i = 0; i < 5; ++i)                                      \
      GLDS16(B + boff[i] + kb_, sb_ + bl[i]);                                          \
  } while (0)
    K2_ISSUE(0, 0);
    K2_ISSUE(1, 1);
    K2_ISSUE(2, 2);
    WAIT_V(26);
    __builtin_amdgcn_s_barrier();
    int buf = 0;
    for (int st = 0; st < 16; ++st) {
      if (st < 14) WAIT_V(13); else WAIT_V(0);
      __builtin_amdgcn_s_barrier();
      if (st + 3 < 16) K2_ISSUE(st + 3, buf);
      buf = buf + 1; if (buf >= 3) buf = 0;
    }
  } else {
    f32x4 acc[2][5];
#pragma unroll
    for (int m = 0; m < 2; ++m)
#pragma unroll
      for (int n = 0; n < 5; ++n) acc[m][n] = (f32x4){0.f, 0.f, 0.f, 0.f};
    const int sw = (r >> 1) & 7;
    const int aro = (w * 32 + r) * 128;
    const int bro = 32768 + r * 128;
    const int co0 = ((0 * 4 + q) ^ sw) << 4, co1 = ((1 * 4 + q) ^ sw) << 4;
    bf16x8 xa[2], xb[5], ya[2], yb[5];
#define K2_READ(fa, fb, sbp, s)                                                                              \
  do {                                                                                                       \
    const int co_ = ((s) & 1) ? co1 : co0;                                                                   \
    _Pragma("unroll") for (int n = 0; n < 5; ++n)                                                            \
      fb[n] = *(const bf16x8*)((sbp) + bro + ((s) >> 1) * 10240 + n * 2048 + co_);                           \
    _Pragma("unroll") for (int m = 0; m < 2; ++m)                                                            \
      fa[m] = *(const bf16x8*)((sbp) + aro + ((s) >> 1) * 16384 + m * 2048 + co_);                           \
  } while (0)
#define K2_MMA(fa, fb)                                                                                       \
  do {                                                                                                       \
    _Pragma("unroll") for (int m = 0; m < 2; ++m)                                                            \
      _Pragma("unroll") for (int n = 0; n < 5; ++n)                                                          \
        acc[m][n] = __builtin_amdgcn_mfma_f32_16x16x32_bf16(fb[n], fa[m], acc[m][n], 0, 0, 0);               \
  } while (0)
    __builtin_amdgcn_s_barrier();
    K2_READ(xa, xb, smem, 0);
    int buf = 0;
    for (int st = 0; st < 16; ++st) {
      const char* sb = smem + buf * K2_STAGE;
      int nb = buf + 1; if (nb >= 3) nb = 0;
      const char* sn = smem + nb * K2_STAGE;
      SB(); K2_READ(ya, yb, sb, 1); SB(); K2_MMA(xa, xb);
      SB(); K2_READ(xa, xb, sb, 2); SB(); K2_MMA(ya, yb);
      SB(); K2_READ(ya, yb, sb, 3); SB(); K2_MMA(xa, xb);
      SB(); WAIT_L(0); __builtin_amdgcn_s_barrier(); SB();
      if (st < 15) K2_READ(xa, xb, sn, 0);
      SB(); K2_MMA(ya, yb);
      buf = nb;
    }
    u16* Y = ypart + (size_t)part * SEQ * DM;
#pragma unroll
    for (int m = 0; m < 2; ++m)
#pragma unroll
      for (int n = 0; n < 5; ++n) {
        u32x2 o;
        o[0] = pk2(acc[m][n][0], acc[m][n][1]);
        o[1] = pk2(acc[m][n][2], acc[m][n][3]);
        *(u32x2*)(Y + (size_t)(t0 + w * 32 + m * 16 + r) * DM + e0 + n * 16 + q * 4) = o;
      }
  }
}

__global__ __launch_bounds__(512) void k3_ln2(const u16* __restrict__ x, const u16* __restrict__ ypart,
                                               const u16* __restrict__ wr, u16* __restrict__ out,
                                               float* __restrict__ rsc, float2* __restrict__ stats) {
  const int lane = threadIdx.x & 63;
  const int row = blockIdx.x * 8 + (threadIdx.x >> 6);
  const unsigned* xp = (const unsigned*)(x + (size_t)row * DM);
  const unsigned* yr = (const unsigned*)(ypart + (size_t)row * DM);
  const unsigned* yi = (const unsigned*)(ypart + (size_t)SEQ * DM + (size_t)row * DM);
  const unsigned* wp = (const unsigned*)wr;
  unsigned xv[5], rv[5], iv[5], wv[5];
#pragma unroll
  for (int i = 0; i < 5; ++i) {
    xv[i] = xp[i * 64 + lane];
    rv[i] = yr[i * 64 + lane];
    iv[i] = yi[i * 64 + lane];
    wv[i] = wp[i * 64 + lane];
  }
  float h[10];
  float s = 0.f;
#pragma unroll
  for (int i = 0; i < 5; ++i) {
    const float y0 = rbf(lo2f(rv[i]) - lo2f(iv[i])), y1 = rbf(hi2f(rv[i]) - hi2f(iv[i]));
    h[2 * i] = rbf(lo2f(xv[i]) + y0);
    h[2 * i + 1] = rbf(hi2f(xv[i]) + y1);
    s += h[2 * i] + h[2 * i + 1];
  }
  s = wave_sum_f32(s);
  const float mean = s * (1.0f / DM);
  const float mb = rbf(mean);
  float d[10];
  float ss = 0.f;
#pragma unroll
  for (int i = 0; i < 10; ++i) {
    d[i] = h[i] - mb;
    ss += d[i] * d[i];
  }
  ss = wave_sum_f32(ss);
  const float dm = mean - mb;
  float var = fmaxf(ss * (1.0f / DM) - dm * dm, 0.f);
  const float vb = rbf(var);
  const float sd = rbf(sqrtf(rbf(vb + 1e-5f)));
  const float rs = 1.0f / sd;
  float dot = 0.f;
  unsigned* op = (unsigned*)(out + (size_t)row * DM);
#pragma unroll
  for (int i = 0; i < 5; ++i) {
    const float n0 = rbf(rbf(d[2 * i]) * rs), n1 = rbf(rbf(d[2 * i + 1]) * rs);
    dot += n0 * lo2f(wv[i]);
    dot += n1 * hi2f(wv[i]);
    op[i * 64 + lane] = pk2(h[2 * i] + n0, h[2 * i + 1] + n1);
  }
  dot = wave_sum_f32(dot);
  if (lane == 0) {
    rsc[row] = rbf(dot);
    stats[row] = make_float2(mb, rs);
  }
}

__device__ __forceinline__ void block_top8(const float* __restrict__ rsc, unsigned* cand  , int* sel  ) {
  const int tid = threadIdx.x, w = tid >> 6, lane = tid & 63;
  unsigned key[8];
  {
    const f32x4 v0 = *(const f32x4*)(rsc + tid * 8);
    const f32x4 v1 = *(const f32x4*)(rsc + tid * 8 + 4);
#pragma unroll
    for (int i = 0; i < 8; ++i) {
      const unsigned u = __float_as_uint(i < 4 ? v0[i] : v1[i - 4]);
      const unsigned o = (u & 0x80000000u) ? ~u : (u | 0x80000000u);
      key[i] = (o & 0xffff0000u) | (0xffffu - (unsigned)(tid * 8 + i));
    }
  }
#pragma unroll
  for (int rnd = 0; rnd < 8; ++rnd) {
    unsigned lm = key[0];
#pragma unroll
    for (int i = 1; i < 8; ++i) lm = max(lm, key[i]);
    const unsigned wm = wave_max_u32(lm);
#pragma unroll
    for (int i = 0; i < 8; ++i) key[i] = (key[i] == wm) ? 0u : key[i];
    if (lane == 0) cand[w * 8 + rnd] = wm;
  }
  __syncthreads();
  if (w == 0) {
    unsigned k2 = (lane < 32) ? cand[lane] : 0u;
#pragma unroll
    for (int rnd = 0; rnd < 8; ++rnd) {
      const unsigned wm = wave_max_u32(k2);
      k2 = (k2 == wm) ? 0u : k2;
      if (lane == 0) sel[rnd] = (int)(0xffffu - (wm & 0xffffu));
    }
  }
  __syncthreads();
}

__device__ __forceinline__ bf16x8 ldtr_frag(const char* tile, int k0, int lane) {
  const int g = lane >> 4, i = lane & 15;
  const char* p = tile + (k0 + 8 * g + (i >> 2)) * 32 + (i & 3) * 8;
  s16x4 lo = __builtin_amdgcn_ds_read_tr16_b64_v4i16((__attribute__((address_space(3))) s16x4*)(p));
  s16x4 hi = __builtin_amdgcn_ds_read_tr16_b64_v4i16((__attribute__((address_space(3))) s16x4*)(p + 128));
  bf16x8 f;
  f[0] = lo[0]; f[1] = lo[1]; f[2] = lo[2]; f[3] = lo[3];
  f[4] = hi[0]; f[5] = hi[1]; f[6] = hi[2]; f[7] = hi[3];
  return f;
}

#define XS_LD 1296
__global__ __launch_bounds__(256) void k4_gateup(const u16* __restrict__ x, const u16* __restrict__ ypart,
                                                  const float* __restrict__ rsc, const float2* __restrict__ stats,
                                                  const u16* __restrict__ wg, const u16* __restrict__ wu,
                                                  u16* __restrict__ act, int* __restrict__ selg) {
  __shared__ __attribute__((aligned(1024))) char smem[20480 * 2 + 16 * XS_LD + 8192 + 128 + 32];
  char* tg = smem;
  char* tu = smem + 20480;
  char* xs = smem + 40960;
  float* red = (float*)(smem + 40960 + 16 * XS_LD);
  unsigned* cand = (unsigned*)(smem + 40960 + 16 * XS_LD + 8192);
  int* sel = (int*)(smem + 40960 + 16 * XS_LD + 8192 + 128);
  const int tid = threadIdx.x, w = tid >> 6, lane = tid & 63, q = lane >> 4, r = lane & 15;
  const int xcd = blockIdx.x & 7, jj = blockIdx.x >> 3;
  const int cg = (jj >> 2) * 32 + xcd * 4 + (jj & 3);
  const int f0 = cg * 16;
#pragma unroll
  for (int i = 0; i < 5; ++i) {
    const int p = w * 5 + i;
    const int row = p * 32 + (lane >> 1);
    GLDS16(wg + (size_t)row * FF + f0 + (lane & 1) * 8, tg + p * 1024);
    GLDS16(wu + (size_t)row * FF + f0 + (lane & 1) * 8, tu + p * 1024);
  }
  block_top8(rsc, cand, sel);
  {
    const int tk = tid >> 5, l32 = tid & 31;
    const int row = sel[tk];
    const float2 st = stats[row];
    const unsigned* xp = (const unsigned*)(x + (size_t)row * DM);
    const unsigned* yr = (const unsigned*)(ypart + (size_t)row * DM);
    const unsigned* yi = (const unsigned*)(ypart + (size_t)SEQ * DM + (size_t)row * DM);
#pragma unroll
    for (int i = 0; i < 10; ++i) {
      const int c = i * 32 + l32;
      const unsigned xv = xp[c], rv = yr[c], iv = yi[c];
      const float h0 = rbf(lo2f(xv) + rbf(lo2f(rv) - lo2f(iv)));
      const float h1 = rbf(hi2f(xv) + rbf(hi2f(rv) - hi2f(iv)));
      *(unsigned*)(xs + tk * XS_LD + c * 4) = pk2(rbf(h0 - st.x) * st.y, rbf(h1 - st.x) * st.y);
      *(unsigned*)(xs + (tk + 8) * XS_LD + c * 4) = 0u;
    }
    if (blockIdx.x == 0 && tid < 8) selg[tid] = sel[tid];
  }
  WAIT_V(0);
  __syncthreads();
  f32x4 ag = {0.f, 0.f, 0.f, 0.f}, au = {0.f, 0.f, 0.f, 0.f};
#pragma unroll
  for (int ks = 0; ks < 5; ++ks) {
    const int k0 = w * 160 + ks * 32;
    const bf16x8 af = *(const bf16x8*)(xs + r * XS_LD + (k0 + q * 8) * 2);
    const bf16x8 bg = ldtr_frag(tg, k0, lane);
    const bf16x8 bu = ldtr_frag(tu, k0, lane);
    ag = __builtin_amdgcn_mfma_f32_16x16x32_bf16(af, bg, ag, 0, 0, 0);
    au = __builtin_amdgcn_mfma_f32_16x16x32_bf16(af, bu, au, 0, 0, 0);
  }
  *(f32x4*)(red + (w * 2 + 0) * 256 + lane * 4) = ag;
  *(f32x4*)(red + (w * 2 + 1) * 256 + lane * 4) = au;
  __syncthreads();
  if (tid < 128) {
    const int tk = tid >> 4, c = tid & 15;
    const int li = ((tk >> 2) * 16 + c) * 4 + (tk & 3);
    float g = 0.f, u = 0.f;
#pragma unroll
    for (int ww = 0; ww < 4; ++ww) {
      g += red[(ww * 2 + 0) * 256 + li];
      u += red[(ww * 2 + 1) * 256 + li];
    }
    g = rbf(g);
    u = rbf(u);
    const float sg = rbf(g / (1.0f + __expf(-g)));
    const unsigned o = pk2(sg * u, 0.f);
    act[tk * FF + f0 + c] = (u16)(o & 0xffffu);
  }
}

#define AC_LD 3088
__global__ __launch_bounds__(256) void k5_down(const u16* __restrict__ x, const u16* __restrict__ ypart,
                                                const float* __restrict__ rsc, const float2* __restrict__ stats,
                                                const u16* __restrict__ wd, const u16* __restrict__ act,
                                                const int* __restrict__ selg, u16* __restrict__ out) {
  __shared__ __attribute__((aligned(1024))) char smem[49152 + 16 * AC_LD + 4096];
  char* td = smem;
  char* as = smem + 49152;
  float* red = (float*)(smem + 49152 + 16 * AC_LD);
  const int tid = threadIdx.x, w = tid >> 6, lane = tid & 63, q = lane >> 4, r = lane & 15;
  const int xcd = blockIdx.x & 7, jj = blockIdx.x >> 3;
  const int cg = xcd * 5 + jj;
  const int c0 = cg * 16;
#pragma unroll
  for (int i = 0; i < 12; ++i) {
    const int p = w * 12 + i;
    const int row = p * 32 + (lane >> 1);
    GLDS16(wd + (size_t)row * DM + c0 + (lane & 1) * 8, td + p * 1024);
  }
#pragma unroll
  for (int i = 0; i < 6; ++i) {
    const int idx = i * 256 + tid;
    const int tk = idx / 192, ch = idx % 192;
    *(u32x4*)(as + tk * AC_LD + ch * 16) = *(const u32x4*)(act + tk * FF + ch * 8);
    *(u32x4*)(as + (tk + 8) * AC_LD + ch * 16) = (u32x4){0u, 0u, 0u, 0u};
  }
  WAIT_V(0);
  __syncthreads();
  f32x4 acc = {0.f, 0.f, 0.f, 0.f};
#pragma unroll
  for (int ks = 0; ks < 12; ++ks) {
    const int k0 = w * 384 + ks * 32;
    const bf16x8 af = *(const bf16x8*)(as + r * AC_LD + (k0 + q * 8) * 2);
    const bf16x8 bd = ldtr_frag(td, k0, lane);
    acc = __builtin_amdgcn_mfma_f32_16x16x32_bf16(af, bd, acc, 0, 0, 0);
  }
  *(f32x4*)(red + w * 256 + lane * 4) = acc;
  __syncthreads();
  if (tid < 128) {
    const int tk = tid >> 4, c = tid & 15;
    const int li = ((tk >> 2) * 16 + c) * 4 + (tk & 3);
    float y = 0.f;
#pragma unroll
    for (int ww = 0; ww < 4; ++ww) y += red[ww * 256 + li];
    y = rbf(y);
    const int row = selg[tk];
    const float rr = rsc[row];
    const float sgm = rbf(1.0f / (1.0f + __expf(-rr)));
    y = rbf(y * sgm);
    const float2 st = stats[row];
    const int col = c0 + c;
    const float xv = bf2f(x[(size_t)row * DM + col]);
    const float yr = bf2f(ypart[(size_t)row * DM + col]);
    const float yi = bf2f(ypart[(size_t)SEQ * DM + (size_t)row * DM + col]);
    const float h = rbf(xv + rbf(yr - yi));
    const float hn = rbf(rbf(h - st.x) * st.y);
    const float bo = rbf(hn + y);
    const unsigned o = pk2(h + bo, 0.f);
    out[(size_t)row * DM + col] = (u16)(o & 0xffffu);
  }
}

extern "C" void kernel_launch(void* const* d_in, const int* in_sizes, int n_in, void* d_out, int out_size,
                              void* d_ws, size_t ws_size, hipStream_t stream) {
  const u16* x = (const u16*)d_in[0];
  const u16* wsre = (const u16*)d_in[1];
  const u16* wsim = (const u16*)d_in[2];
  const u16* wdre = (const u16*)d_in[3];
  const u16* wdim = (const u16*)d_in[4];
  const u16* wr = (const u16*)d_in[6];
  const u16* wg = (const u16*)d_in[7];
  const u16* wu = (const u16*)d_in[8];
  const u16* wdn = (const u16*)d_in[9];
  u16* out = (u16*)d_out;
  char* ws = (char*)d_ws;
  u16* abT = (u16*)ws;
  u16* ypart = (u16*)(ws + 5242880);
  float* rsc = (float*)(ws + 10485760);
  float2* stats = (float2*)(ws + 10485760 + 8192);
  u16* act = (u16*)(ws + 10485760 + 8192 + 16384);
  int* selg = (int*)(ws + 10485760 + 8192 + 16384 + 24576);

  k1_ln_gemm<<<256, 576, 0, stream>>>(x, wdre, wdim, abT, wsre, wsim, wg, wu, wdn);
  k2_gemm<<<256, 512, 0, stream>>>(wsre, wsim, abT, ypart);
  k3_ln2<<<256, 512, 0, stream>>>(x, ypart, wr, out, rsc, stats);
  k4_gateup<<<96, 256, 0, stream>>>(x, ypart, rsc, stats, wg, wu, act, selg);
  k5_down<<<40, 256, 0, stream>>>(x, ypart, rsc, stats, wdn, act, selg, out);
}
```

```cpp
#include <hip/hip_runtime.h>
#include <hip/hip_bf16.h>
#include <stdint.h>

#define K1_PREFETCH 1
#define SEQ 2048
#define DM 640
#define FF 1536
#define TOPC 8

typedef unsigned short u16;
typedef __attribute__((ext_vector_type(8))) short bf16x8;
typedef __attribute__((ext_vector_type(4))) short s16x4;
typedef __attribute__((ext_vector_type(4))) float f32x4;
typedef __attribute__((ext_vector_type(2))) float f32x2;
typedef __attribute__((ext_vector_type(2))) __bf16 bf16x2_t;
typedef __attribute__((ext_vector_type(4))) unsigned u32x4;
typedef __attribute__((ext_vector_type(2))) unsigned u32x2;

__device__ __forceinline__ unsigned pk2(float a, float b) {
  f32x2 v = {a, b};
  bf16x2_t r = __builtin_convertvector(v, bf16x2_t);
  return __builtin_bit_cast(unsigned, r);
}
__device__ __forceinline__ float lo2f(unsigned w) { return __uint_as_float(w << 16); }
__device__ __forceinline__ float hi2f(unsigned w) { return __uint_as_float(w & 0xffff0000u); }
__device__ __forceinline__ float rbf(float a) { return lo2f(pk2(a, 0.f)); }
__device__ __forceinline__ float bf2f(u16 u) { return __uint_as_float(((unsigned)u) << 16); }

#define WAIT_V(n) asm volatile("s_waitcnt vmcnt(%0)" ::"n"(n) : "memory")
#define WAIT_L(n) asm volatile("s_waitcnt lgkmcnt(%0)" ::"n"(n) : "memory")
#define RAW_BARRIER() do { WAIT_L(0); __builtin_amdgcn_s_barrier(); } while (0)

#define GLDS16(gptr, lptr) __builtin_amdgcn_global_load_lds((const unsigned*)(gptr), (unsigned*)(lptr), 16, 0, 0)

__device__ __forceinline__ void glds16_asm(const void* gsrc, unsigned lds_dst) {
  unsigned keep;
  asm volatile("s_mov_b32 %0, m0\n\ts_mov_b32 m0, %2\n\ts_nop 0\n\tglobal_load_lds_dwordx4 %1, off\n\ts_mov_b32 m0, %0"
               : "=&s"(keep) : "v"(gsrc), "s"(lds_dst) : "memory");
}
__device__ __forceinline__ unsigned lds_addr(const char* p) {
  return (unsigned)(uintptr_t)(const __attribute__((address_space(3))) char*)p;
}

__device__ __forceinline__ unsigned wave_max_u32(unsigned v) {
  v = max(v, (unsigned)__builtin_amdgcn_update_dpp(0, (int)v, 0x111, 0xf, 0xf, false));
  v = max(v, (unsigned)__builtin_amdgcn_update_dpp(0, (int)v, 0x112, 0xf, 0xf, false));
  v = max(v, (unsigned)__builtin_amdgcn_update_dpp(0, (int)v, 0x114, 0xf, 0xf, false));
  v = max(v, (unsigned)__builtin_amdgcn_update_dpp(0, (int)v, 0x118, 0xf, 0xf, false));
  v = max(v, (unsigned)__builtin_amdgcn_update_dpp(0, (int)v, 0x142, 0xa, 0xf, false));
  v = max(v, (unsigned)__builtin_amdgcn_update_dpp(0, (int)v, 0x143, 0xc, 0xf, false));
  return (unsigned)__builtin_amdgcn_readlane((int)v, 63);
}
__device__ __forceinline__ float dpp_f(float v, int ctrl_dummy);
template <int CTRL, int RM>
__device__ __forceinline__ float dppf(float v) {
  return __int_as_float(__builtin_amdgcn_update_dpp(0, __float_as_int(v), CTRL, RM, 0xf, false));
}
__device__ __forceinline__ float wave_sum_f32(float v) {
  v += dppf<0x111, 0xf>(v);
  v += dppf<0x112, 0xf>(v);
  v += dppf<0x114, 0xf>(v);
  v += dppf<0x118, 0xf>(v);
  v += dppf<0x142, 0xa>(v);
  v += dppf<0x143, 0xc>(v);
  return __int_as_float(__builtin_amdgcn_readlane(__float_as_int(v), 63));
}

#define K1_AOFF 0
#define K1_BOFF 81920
#define K1_BST 20480
#define K1_DUMMY (81920 + 3 * 20480)
#define K1_TLD 144
__device__ __forceinline__ float red8(float v) {
  v += dppf<0xB1, 0xf>(v);
  v += dppf<0x4E, 0xf>(v);
  v += dppf<0x141, 0xf>(v);
  return v;
}
__device__ __forceinline__ void k1_ln_rows(const u32x4 (&a)[10], char* atile, int row, int c8) {
  f32x2 v[40];
  f32x2 s2 = {0.f, 0.f};
#pragma unroll
  for (int jj = 0; jj < 10; ++jj)
#pragma unroll
    for (int c = 0; c < 4; ++c) {
      v[jj * 4 + c] = (f32x2){lo2f(a[jj][c]), hi2f(a[jj][c])};
      s2 += v[jj * 4 + c];
    }
  const float s = red8(s2[0] + s2[1]);
  const float mean = s * (1.0f / DM);
  const float mb = rbf(mean);
  const f32x2 mb2 = {mb, mb};
  f32x2 q2 = {0.f, 0.f};
#pragma unroll
  for (int i = 0; i < 40; ++i) {
    const f32x2 d = v[i] - mb2;
    q2 = __builtin_elementwise_fma(d, d, q2);
    const unsigned u = pk2(d[0], d[1]);
    v[i] = (f32x2){lo2f(u), hi2f(u)};
  }
  const float ss = red8(q2[0] + q2[1]);
  const float dm = mean - mb;
  const float var = fmaxf(ss * (1.0f / DM) - dm * dm, 0.f);
  const float vb = rbf(var);
  const float sd = rbf(sqrtf(rbf(vb + 1e-5f)));
  const float rs = 1.0f / sd;
  const f32x2 rs2 = {rs, rs};
  char* ap = atile + row * 128 + ((c8 ^ ((row >> 1) & 7)) << 4);
#pragma unroll
  for (int jj = 0; jj < 10; ++jj) {
    u32x4 o;
#pragma unroll
    for (int c = 0; c < 4; ++c) {
      const f32x2 n = v[jj * 4 + c] * rs2;
      o[c] = pk2(n[0], n[1]);
    }
    *(u32x4*)(ap + jj * 8192) = o;
  }
}
__global__ __launch_bounds__(512) void k1_ln_gemm(const u16* __restrict__ x, const u16* __restrict__ wre,
                                                   const u16* __restrict__ wim, u16* __restrict__ abT,
                                                   const u16* __restrict__ wsre, const u16* __restrict__ wsim) {
  __shared__ __attribute__((aligned(1024))) char smem[K1_DUMMY + 1024];
  const int tid = threadIdx.x, lane = tid & 63, q = lane >> 4, r = lane & 15;
  const int w = __builtin_amdgcn_readfirstlane(tid >> 6);
  const int xcd = blockIdx.x & 7, j = blockIdx.x >> 3;
  const int mt = (xcd >> 1) * 8 + (j & 7);
  const int nt = (xcd & 1) * 4 + (j >> 3);
  const int s0 = mt * 64;

  const int row8 = lane >> 3, c8 = lane & 7;
  const u16* W = (nt < 4) ? (wre + (size_t)nt * 160 * DM) : (wim + (size_t)(nt - 4) * 160 * DM);
  const int lw = w - 4;
  const char* pfp = (const char*)((xcd >> 2) ? wsim : wsre) + (size_t)(xcd & 3) * 512 * SEQ * 2 + (size_t)j * 65536 + lane * 16;
  const u16* xr = x + (size_t)(s0 + w * 8 + row8) * DM + c8 * 8;
#define K1_XLOAD()                                                                                  \
  _Pragma("unroll") for (int jj = 0; jj < 10; ++jj)                                                 \
    asm volatile("global_load_dwordx4 %0, %1, off offset:%2" : "=v"(a[jj]) : "v"(xr), "n"(jj * 128) : "memory")
#define K1_XWAIT(n)                                                                                 \
  asm volatile("s_waitcnt vmcnt(" #n ")"                                                            \
               : "+v"(a[0]), "+v"(a[1]), "+v"(a[2]), "+v"(a[3]), "+v"(a[4]), "+v"(a[5]), "+v"(a[6]), "+v"(a[7]), "+v"(a[8]), "+v"(a[9]) \
               :: "memory")
  if (w >= 4) {
    u32x4 a[10];
    K1_XLOAD();
    int boff[5];
#pragma unroll
    for (int i = 0; i < 5; ++i) {
      const int row = (lw * 5 + i) * 8 + row8;
      boff[i] = row * DM + ((c8 ^ ((row >> 1) & 7)) << 3);
    }
#define K1_ISSUE(stage, buf)                                                                        \
  do {                                                                                              \
    _Pragma("unroll") for (int i = 0; i < 5; ++i)                                                   \
      glds16_asm(W + boff[i] + (stage) * 64, lds_addr(smem) + K1_BOFF + (buf) * K1_BST + (lw * 5 + i) * 1024); \
  } while (0)
    K1_ISSUE(0, 0);
    K1_ISSUE(1, 1);
    K1_ISSUE(2, 2);
    K1_XWAIT(15);
    k1_ln_rows(a, smem + K1_AOFF, w * 8 + row8, c8);
    WAIT_V(10);
    WAIT_L(0);
    __builtin_amdgcn_s_barrier();
#pragma unroll
    for (int st = 0; st < 10; ++st) {
      if (st < 8) WAIT_V(5); else WAIT_V(0);
      __builtin_amdgcn_s_barrier();
      if (st + 3 < 10) K1_ISSUE(st + 3, st % 3);
    }
  } else {
    u32x4 a[10];
    K1_XLOAD();
    K1_XWAIT(0);
    k1_ln_rows(a, smem + K1_AOFF, w * 8 + row8, c8);
#pragma unroll
    for (int i = 0; i < 16; ++i) glds16_asm(pfp + (w * 16 + i) * 1024, lds_addr(smem) + K1_DUMMY);
    const int wm = w >> 1, wn = w & 1;
    f32x4 acc[2][5];
#pragma unroll
    for (int m = 0; m < 2; ++m)
#pragma unroll
      for (int n = 0; n < 5; ++n) acc[m][n] = (f32x4){0.f, 0.f, 0.f, 0.f};
    const int sw = (r >> 1) & 7;
    const int aro = K1_AOFF + (wm * 32 + r) * 128;
    const int bro = K1_BOFF + (wn * 80 + r) * 128;
    const int co0 = ((0 * 4 + q) ^ sw) << 4, co1 = ((1 * 4 + q) ^ sw) << 4;
    bf16x8 xa[2], xb[5], ya[2], yb[5];
#define K1_READ(fa, fb, st, h)                                                                      \
  do {                                                                                              \
    const int co_ = (h) ? co1 : co0;                                                                \
    _Pragma("unroll") for (int n = 0; n < 5; ++n)                                                   \
      fb[n] = *(const bf16x8*)(smem + bro + ((st) % 3) * K1_BST + n * 2048 + co_);                  \
    _Pragma("unroll") for (int m = 0; m < 2; ++m)                                                   \
      fa[m] = *(const bf16x8*)(smem + aro + (st) * 8192 + m * 2048 + co_);                          \
  } while (0)
#define K1_MMA(fa, fb)                                                                              \
  do {                                                                                              \
    _Pragma("unroll") for (int m = 0; m < 2; ++m)                                                   \
      _Pragma("unroll") for (int n = 0; n < 5; ++n)                                                 \
        acc[m][n] = __builtin_amdgcn_mfma_f32_16x16x32_bf16(fa[m], fb[n], acc[m][n], 0, 0, 0);      \
  } while (0)
#define SB() __builtin_amdgcn_sched_barrier(0)
    WAIT_L(0);
    __builtin_amdgcn_s_barrier();
    K1_READ(xa, xb, 0, 0);
#pragma unroll
    for (int st = 0; st < 10; ++st) {
      SB(); K1_READ(ya, yb, st, 1); SB(); K1_MMA(xa, xb);
      SB(); WAIT_L(0); __builtin_amdgcn_s_barrier(); SB();
      if (st < 9) K1_READ(xa, xb, st + 1, 0);
      SB(); K1_MMA(ya, yb);
    }
#pragma unroll
    for (int m = 0; m < 2; ++m)
#pragma unroll
      for (int n = 0; n < 5; ++n) {
        u32x2 o;
        o[0] = pk2(acc[m][n][0], acc[m][n][1]);
        o[1] = pk2(acc[m][n][2], acc[m][n][3]);
        *(u32x2*)(smem + (wn * 80 + n * 16 + r) * K1_TLD + (wm * 32 + m * 16 + q * 4) * 2) = o;
      }
  }
  WAIT_L(0);
  __builtin_amdgcn_s_barrier();
#pragma unroll
  for (int i = 0; i < 3; ++i) {
    const int c = tid + i * 512;
    if (c < 1280) {
      const int e = c >> 3, p = c & 7;
      const u32x4 o = *(const u32x4*)(smem + e * K1_TLD + p * 16);
      *(u32x4*)(abT + (size_t)(nt * 160 + e) * SEQ + s0 + p * 8) = o;
    }
  }
  WAIT_V(0);
}

#define K2_STAGE 53248
#define K2_TLD 176
__global__ __launch_bounds__(512) void k2_gemm(const u16* __restrict__ wsre, const u16* __restrict__ wsim,
                                                const u16* __restrict__ abT, u16* __restrict__ ypart) {
  __shared__ __attribute__((aligned(1024))) char smem[3 * K2_STAGE];
  const int tid = threadIdx.x, lane = tid & 63, q = lane >> 4, r = lane & 15;
  const int w = __builtin_amdgcn_readfirstlane(tid >> 6);
  const int xcd = blockIdx.x & 7, j = blockIdx.x >> 3;
  const int part = xcd >> 2;
  const int mt = (xcd & 3) * 4 + (j & 3);
  const int nt = j >> 2;
  const int t0 = mt * 128, e0 = nt * 80;

  if (w >= 4) {
    const int lw = w - 4;
    const u16* A = (part ? wsim : wsre) + (size_t)t0 * SEQ;
    const u16* B = abT + (size_t)(part * DM + e0) * SEQ;
    const int rin = lane >> 3, pos = lane & 7;
    int aoff[4], boff[5], bl[5];
#pragma unroll
    for (int p = 0; p < 4; ++p) {
      const int row = lw * 32 + p * 8 + rin;
      aoff[p] = row * SEQ + ((pos ^ ((row >> 1) & 7)) << 3);
    }
#pragma unroll
    for (int i = 0; i < 5; ++i) {
      const int pc = lw * 5 + i;
      const int sub = pc / 10, p = pc % 10;
      const int row = p * 8 + rin;
      boff[i] = row * SEQ + sub * 64 + ((pos ^ ((row >> 1) & 7)) << 3);
      bl[i] = 32768 + sub * 10240 + p * 1024;
    }
#define K2_ISSUE(stage, buf)                                                           \
  do {                                                                                 \
    char* sb_ = smem + (buf) * K2_STAGE;                                               \
    const int kb_ = (stage) * 128;                                                     \
    _Pragma("unroll") for (int sub = 0; sub < 2; ++sub)                                \
      _Pragma("unroll") for (int p = 0; p < 4; ++p)                                    \
        GLDS16(A + aoff[p] + kb_ + sub * 64, sb_ + sub * 16384 + (lw * 4 + p) * 1024); \
    _Pragma("unroll") for (int i = 0; i < 5; ++i)                                      \
      GLDS16(B + boff[i] + kb_, sb_ + bl[i]);                                          \
  } while (0)
    K2_ISSUE(0, 0);
    K2_ISSUE(1, 1);
    K2_ISSUE(2, 2);
    WAIT_V(26);
    __builtin_amdgcn_s_barrier();
    int buf = 0;
    for (int st = 0; st < 16; ++st) {
      if (st < 14) WAIT_V(13); else WAIT_V(0);
      __builtin_amdgcn_s_barrier();
      if (st + 3 < 16) K2_ISSUE(st + 3, buf);
      buf = buf + 1; if (buf >= 3) buf = 0;
    }
  } else {
    f32x4 acc[2][5];
#pragma unroll
    for (int m = 0; m < 2; ++m)
#pragma unroll
      for (int n = 0; n < 5; ++n) acc[m][n] = (f32x4){0.f, 0.f, 0.f, 0.f};
    const int sw = (r >> 1) & 7;
    const int aro = (w * 32 + r) * 128;
    const int bro = 32768 + r * 128;
    const int co0 = ((0 * 4 + q) ^ sw) << 4, co1 = ((1 * 4 + q) ^ sw) << 4;
    bf16x8 xa[2], xb[5], ya[2], yb[5];
#define K2_READ(fa, fb, sbp, s)                                                                              \
  do {                                                                                                       \
    const int co_ = ((s) & 1) ? co1 : co0;                                                                   \
    _Pragma("unroll") for (int n = 0; n < 5; ++n)                                                            \
      fb[n] = *(const bf16x8*)((sbp) + bro + ((s) >> 1) * 10240 + n * 2048 + co_);                           \
    _Pragma("unroll") for (int m = 0; m < 2; ++m)                                                            \
      fa[m] = *(const bf16x8*)((sbp) + aro + ((s) >> 1) * 16384 + m * 2048 + co_);                           \
  } while (0)
#define K2_MMA(fa, fb)                                                                                       \
  do {                                                                                                       \
    _Pragma("unroll") for (int m = 0; m < 2; ++m)                                                            \
      _Pragma("unroll") for (int n = 0; n < 5; ++n)                                                          \
        acc[m][n] = __builtin_amdgcn_mfma_f32_16x16x32_bf16(fb[n], fa[m], acc[m][n], 0, 0, 0);               \
  } while (0)
    __builtin_amdgcn_s_barrier();
    K2_READ(xa, xb, smem, 0);
    int buf = 0;
    for (int st = 0; st < 16; ++st) {
      const char* sb = smem + buf * K2_STAGE;
      int nb = buf + 1; if (nb >= 3) nb = 0;
      const char* sn = smem + nb * K2_STAGE;
      SB(); K2_READ(ya, yb, sb, 1); SB(); K2_MMA(xa, xb);
      SB(); K2_READ(xa, xb, sb, 2); SB(); K2_MMA(ya, yb);
      SB(); K2_READ(ya, yb, sb, 3); SB(); K2_MMA(xa, xb);
      SB(); WAIT_L(0); __builtin_amdgcn_s_barrier(); SB();
      if (st < 15) K2_READ(xa, xb, sn, 0);
      SB(); K2_MMA(ya, yb);
      buf = nb;
    }
#pragma unroll
    for (int m = 0; m < 2; ++m)
#pragma unroll
      for (int n = 0; n < 5; ++n) {
        u32x2 o;
        o[0] = pk2(acc[m][n][0], acc[m][n][1]);
        o[1] = pk2(acc[m][n][2], acc[m][n][3]);
        *(u32x2*)(smem + (w * 32 + m * 16 + r) * K2_TLD + (n * 16 + q * 4) * 2) = o;
      }
  }
  WAIT_L(0);
  __builtin_amdgcn_s_barrier();
  {
    u16* Y = ypart + (size_t)part * SEQ * DM;
#pragma unroll
    for (int i = 0; i < 3; ++i) {
      const int c = tid + i * 512;
      if (c < 1280) {
        const int t = c / 10, p = c % 10;
        const u32x4 o = *(const u32x4*)(smem + t * K2_TLD + p * 16);
        *(u32x4*)(Y + (size_t)(t0 + t) * DM + e0 + p * 8) = o;
      }
    }
  }
}

__global__ __launch_bounds__(576) void k3_ln2(const u16* __restrict__ x, const u16* __restrict__ ypart,
                                               const u16* __restrict__ wr, u16* __restrict__ out,
                                               float* __restrict__ rsc, float2* __restrict__ stats,
                                               const u16* __restrict__ wg, const u16* __restrict__ wu,
                                               const u16* __restrict__ wdn) {
  const int lane = threadIdx.x & 63;
  if (__builtin_amdgcn_readfirstlane(threadIdx.x >> 6) == 8) {
    __shared__ __attribute__((aligned(1024))) char dl[1024];
    const size_t fo = (size_t)blockIdx.x * 7680 + lane * 16;
#pragma unroll
    for (int i = 0; i < 7; ++i) {
      GLDS16((const char*)wg + fo + i * 1024, dl);
      GLDS16((const char*)wu + fo + i * 1024, dl);
      GLDS16((const char*)wdn + fo + i * 1024, dl);
    }
    if (lane < 32) {
      GLDS16((const char*)wg + fo + 7168, dl);
      GLDS16((const char*)wu + fo + 7168, dl);
      GLDS16((const char*)wdn + fo + 7168, dl);
    }
    WAIT_V(0);
    return;
  }
  const int row = blockIdx.x * 8 + (threadIdx.x >> 6);
  const unsigned* xp = (const unsigned*)(x + (size_t)row * DM);
  const unsigned* yr = (const unsigned*)(ypart + (size_t)row * DM);
  const unsigned* yi = (const unsigned*)(ypart + (size_t)SEQ * DM + (size_t)row * DM);
  const unsigned* wp = (const unsigned*)wr;
  unsigned xv[5], rv[5], iv[5], wv[5];
#pragma unroll
  for (int i = 0; i < 5; ++i) {
    xv[i] = xp[i * 64 + lane];
    rv[i] = yr[i * 64 + lane];
    iv[i] = yi[i * 64 + lane];
    wv[i] = wp[i * 64 + lane];
  }
  float h[10];
  float s = 0.f;
#pragma unroll
  for (int i = 0; i < 5; ++i) {
    const float y0 = rbf(lo2f(rv[i]) - lo2f(iv[i])), y1 = rbf(hi2f(rv[i]) - hi2f(iv[i]));
    h[2 * i] = rbf(lo2f(xv[i]) + y0);
    h[2 * i + 1] = rbf(hi2f(xv[i]) + y1);
    s += h[2 * i] + h[2 * i + 1];
  }
  s = wave_sum_f32(s);
  const float mean = s * (1.0f / DM);
  const float mb = rbf(mean);
  float d[10];
  float ss = 0.f;
#pragma unroll
  for (int i = 0; i < 10; ++i) {
    d[i] = h[i] - mb;
    ss += d[i] * d[i];
  }
  ss = wave_sum_f32(ss);
  const float dm = mean - mb;
  float var = fmaxf(ss * (1.0f / DM) - dm * dm, 0.f);
  const float vb = rbf(var);
  const float sd = rbf(sqrtf(rbf(vb + 1e-5f)));
  const float rs = 1.0f / sd;
  float dot = 0.f;
  unsigned* op = (unsigned*)(out + (size_t)row * DM);
#pragma unroll
  for (int i = 0; i < 5; ++i) {
    const float n0 = rbf(rbf(d[2 * i]) * rs), n1 = rbf(rbf(d[2 * i + 1]) * rs);
    dot += n0 * lo2f(wv[i]);
    dot += n1 * hi2f(wv[i]);
    op[i * 64 + lane] = pk2(h[2 * i] + n0, h[2 * i + 1] + n1);
  }
  dot = wave_sum_f32(dot);
  if (lane == 0) {
    rsc[row] = rbf(dot);
    stats[row] = make_float2(mb, rs);
  }
}

__device__ __forceinline__ bf16x8 ldtr_frag(const char* tile, int k0, int lane) {
  const int g = lane >> 4, i = lane & 15;
  const char* p = tile + (k0 + 8 * g + (i >> 2)) * 32 + (i & 3) * 8;
  s16x4 lo = __builtin_amdgcn_ds_read_tr16_b64_v4i16((__attribute__((address_space(3))) s16x4*)(p));
  s16x4 hi = __builtin_amdgcn_ds_read_tr16_b64_v4i16((__attribute__((address_space(3))) s16x4*)(p + 128));
  bf16x8 f;
  f[0] = lo[0]; f[1] = lo[1]; f[2] = lo[2]; f[3] = lo[3];
  f[4] = hi[0]; f[5] = hi[1]; f[6] = hi[2]; f[7] = hi[3];
  return f;
}

#define XS_LD 1296
#define K4_XS 40960
#define K4_RED (40960 + 8 * XS_LD)
#define K4_CAND (K4_RED + 4096)
__global__ __launch_bounds__(576) void k4_gateup(const u16* __restrict__ x, const u16* __restrict__ ypart,
                                                  const float* __restrict__ rsc, const float2* __restrict__ stats,
                                                  const u16* __restrict__ wg, const u16* __restrict__ wu,
                                                  unsigned* __restrict__ act, unsigned* __restrict__ hsel,
                                                  int* __restrict__ selg, float* __restrict__ gsel,
                                                  const u16* __restrict__ wd, char* __restrict__ wdt) {
  __shared__ __attribute__((aligned(1024))) char smem[K4_CAND + 256];
  const int tid = threadIdx.x, lane = tid & 63, q = lane >> 4, r = lane & 15;
  const int w = __builtin_amdgcn_readfirstlane(tid >> 6);
  if (blockIdx.x >= 96) {
    if (w == 8) return;
    const int b2 = blockIdx.x - 96;
    const char* srcp = (const char*)wd + (size_t)b2 * 16 * 1280;
    u32x4 v[3];
#pragma unroll
    for (int i = 0; i < 3; ++i) { const int c = i * 512 + tid; if (c < 1280) v[i] = *(const u32x4*)(srcp + c * 16); }
#pragma unroll
    for (int i = 0; i < 3; ++i) { const int c = i * 512 + tid; if (c < 1280) *(u32x4*)(smem + (c / 80) * 1296 + (c % 80) * 16) = v[i]; }
    WAIT_L(0);
    __builtin_amdgcn_s_barrier();
#pragma unroll
    for (int i = 0; i < 3; ++i) {
      const int d = i * 512 + tid;
      if (d < 1280) {
        const int cg = d >> 5, rw = (d & 31) >> 1, hf = d & 1;
        const u32x4 o = *(const u32x4*)(smem + rw * 1296 + cg * 32 + hf * 16);
        *(u32x4*)(wdt + (size_t)cg * 49152 + (b2 * 16 + rw) * 32 + hf * 16) = o;
      }
    }
    return;
  }
  const int xcd = blockIdx.x & 7, jj = blockIdx.x >> 3;
  const int f0 = ((jj >> 2) * 32 + xcd * 4 + (jj & 3)) * 16;
  if (w == 8) {
#pragma unroll 4
    for (int p = 0; p < 20; ++p) {
      const int row = p * 32 + (lane >> 1);
      GLDS16(wg + (size_t)row * FF + f0 + (lane & 1) * 8, smem + p * 1024);
      GLDS16(wu + (size_t)row * FF + f0 + (lane & 1) * 8, smem + 20480 + p * 1024);
    }
    __builtin_amdgcn_s_barrier();
    WAIT_V(0);
    return;
  }
  unsigned key[4];
  {
    const u32x4 v = *(const u32x4*)(rsc + tid * 4);
#pragma unroll
    for (int i = 0; i < 4; ++i) {
      const unsigned u = v[i];
      const unsigned o = (u & 0x80000000u) ? ~u : (u | 0x80000000u);
      key[i] = (o & 0xffff0000u) | (0xffffu - (unsigned)(tid * 4 + i));
    }
  }
  unsigned* cand = (unsigned*)(smem + K4_CAND);
#pragma unroll
  for (int rnd = 0; rnd < 8; ++rnd) {
    const unsigned lm = max(max(key[0], key[1]), max(key[2], key[3]));
    const unsigned wm = wave_max_u32(lm);
#pragma unroll
    for (int i = 0; i < 4; ++i) key[i] = (key[i] == wm) ? 0u : key[i];
    if (lane == 0) cand[w * 8 + rnd] = wm;
  }
  WAIT_L(0);
  __builtin_amdgcn_s_barrier();
  unsigned myk = 0;
  {
    unsigned k2 = cand[lane];
#pragma unroll
    for (int rnd = 0; rnd < 8; ++rnd) {
      const unsigned wm = wave_max_u32(k2);
      k2 = (k2 == wm) ? 0u : k2;
      myk = (w == rnd) ? wm : myk;
    }
  }
  {
    const int row = (int)(0xffffu - (myk & 0xffffu));
    const float2 st = stats[row];
    const unsigned* xp = (const unsigned*)(x + (size_t)row * DM);
    const unsigned* yr = (const unsigned*)(ypart + (size_t)row * DM);
    const unsigned* yi = (const unsigned*)(ypart + (size_t)SEQ * DM + (size_t)row * DM);
    unsigned xv[5], rv[5], iv[5];
#pragma unroll
    for (int i = 0; i < 5; ++i) { xv[i] = xp[i * 64 + lane]; rv[i] = yr[i * 64 + lane]; iv[i] = yi[i * 64 + lane]; }
#pragma unroll
    for (int i = 0; i < 5; ++i) {
      const float h0 = rbf(lo2f(xv[i]) + rbf(lo2f(rv[i]) - lo2f(iv[i])));
      const float h1 = rbf(hi2f(xv[i]) + rbf(hi2f(rv[i]) - hi2f(iv[i])));
      const unsigned hn = pk2(rbf(h0 - st.x) * st.y, rbf(h1 - st.x) * st.y);
      *(unsigned*)(smem + K4_XS + w * XS_LD + (i * 64 + lane) * 4) = hn;
      if (blockIdx.x == 0) {
        hsel[w * 320 + i * 64 + lane] = pk2(h0, h1);
        hsel[2560 + w * 320 + i * 64 + lane] = hn;
      }
    }
    if (blockIdx.x == 0 && lane == 0) {
      const unsigned ob = myk & 0xffff0000u;
      const float rr = __uint_as_float((ob & 0x80000000u) ? (ob & 0x7fffffffu) : (~ob & 0xffff0000u));
      selg[w] = row;
      gsel[w] = rbf(1.0f / (1.0f + __expf(-rr)));
    }
  }
  WAIT_L(0);
  __builtin_amdgcn_s_barrier();
  float* red = (float*)(smem + K4_RED);
  {
    const char* tile = smem + (w >> 2) * 20480;
    f32x4 acc = {0.f, 0.f, 0.f, 0.f};
#pragma unroll
    for (int ks = 0; ks < 5; ++ks) {
      const int k0 = (w & 3) * 160 + ks * 32;
      bf16x8 af = *(const bf16x8*)(smem + K4_XS + (r & 7) * XS_LD + (k0 + q * 8) * 2);
      if (r >= 8) af = (bf16x8){0, 0, 0, 0, 0, 0, 0, 0};
      const bf16x8 bw = ldtr_frag(tile, k0, lane);
      acc = __builtin_amdgcn_mfma_f32_16x16x32_bf16(af, bw, acc, 0, 0, 0);
    }
    if (lane < 32) *(f32x4*)(red + w * 128 + lane * 4) = acc;
  }
  WAIT_L(0);
  __builtin_amdgcn_s_barrier();
  if (tid < 64) {
    const int tk = tid >> 3, pr = tid & 7;
    float av[2];
#pragma unroll
    for (int e = 0; e < 2; ++e) {
      const int li = ((tk >> 2) * 16 + pr * 2 + e) * 4 + (tk & 3);
      float g = 0.f, u = 0.f;
#pragma unroll
      for (int ww = 0; ww < 4; ++ww) {
        g += red[ww * 128 + li];
        u += red[(ww + 4) * 128 + li];
      }
      g = rbf(g);
      u = rbf(u);
      const float sg = rbf(g / (1.0f + __expf(-g)));
      av[e] = sg * u;
    }
    act[tk * 768 + (f0 >> 1) + pr] = pk2(av[0], av[1]);
  }
}

#define AC_LD 3088
#define K5_AS 49152
#define K5_RED (49152 + 8 * AC_LD)
__global__ __launch_bounds__(576) void k5_down(const char* __restrict__ wdt, const u32x4* __restrict__ act,
                                                const unsigned* __restrict__ hsel, const int* __restrict__ selg,
                                                const float* __restrict__ gsel, u16* __restrict__ out) {
  __shared__ __attribute__((aligned(1024))) char smem[K5_RED + 4096];
  const int tid = threadIdx.x, lane = tid & 63, q = lane >> 4, r = lane & 15;
  const int w = __builtin_amdgcn_readfirstlane(tid >> 6);
  const int xcd = blockIdx.x & 7, jj = blockIdx.x >> 3;
  const int c0 = (xcd * 5 + jj) * 16;
  if (w == 8) {
#pragma unroll 4
    for (int p = 0; p < 48; ++p) GLDS16(wdt + (size_t)(c0 >> 4) * 49152 + p * 1024 + lane * 16, smem + p * 1024);
    WAIT_V(0);
    return;
  }
  u32x4 av[3];
#pragma unroll
  for (int i = 0; i < 3; ++i) av[i] = act[i * 512 + tid];
  const int tk = (tid >> 3) & 7, pr = tid & 7;
  const unsigned hv = hsel[tk * 320 + (c0 >> 1) + pr];
  const unsigned nv = hsel[2560 + tk * 320 + (c0 >> 1) + pr];
  const int orow = selg[tk];
  const float sgm = gsel[tk];
#pragma unroll
  for (int i = 0; i < 3; ++i) {
    const int idx = i * 512 + tid;
    *(u32x4*)(smem + K5_AS + (idx / 192) * AC_LD + (idx % 192) * 16) = av[i];
  }
  WAIT_L(0);
  __builtin_amdgcn_s_barrier();
  float* red = (float*)(smem + K5_RED);
  {
    f32x4 acc = {0.f, 0.f, 0.f, 0.f};
#pragma unroll
    for (int ks = 0; ks < 6; ++ks) {
      const int k0 = w * 192 + ks * 32;
      bf16x8 af = *(const bf16x8*)(smem + K5_AS + (r & 7) * AC_LD + (k0 + q * 8) * 2);
      if (r >= 8) af = (bf16x8){0, 0, 0, 0, 0, 0, 0, 0};
      const bf16x8 bw = ldtr_frag(smem, k0, lane);
      acc = __builtin_amdgcn_mfma_f32_16x16x32_bf16(af, bw, acc, 0, 0, 0);
    }
    if (lane < 32) *(f32x4*)(red + w * 128 + lane * 4) = acc;
  }
  WAIT_L(0);
  __builtin_amdgcn_s_barrier();
  if (tid < 64) {
    float ov[2];
#pragma unroll
    for (int e = 0; e < 2; ++e) {
      const int li = ((tk >> 2) * 16 + pr * 2 + e) * 4 + (tk & 3);
      float y = 0.f;
#pragma unroll
      for (int ww = 0; ww < 8; ++ww) y += red[ww * 128 + li];
      y = rbf(rbf(y) * sgm);
      const float h = e ? hi2f(hv) : lo2f(hv), hn = e ? hi2f(nv) : lo2f(nv);
      ov[e] = h + rbf(hn + y);
    }
    *(unsigned*)(out + (size_t)orow * DM + c0 + pr * 2) = pk2(ov[0], ov[1]);
  }
}

extern "C" void kernel_launch(void* const* d_in, const int* in_sizes, int n_in, void* d_out, int out_size,
                              void* d_ws, size_t ws_size, hipStream_t stream) {
  const u16* x = (const u16*)d_in[0];
  const u16* wsre = (const u16*)d_in[1];
  const u16* wsim = (const u16*)d_in[2];
  const u16* wdre = (const u16*)d_in[3];
  const u16* wdim = (const u16*)d_in[4];
  const u16* wr = (const u16*)d_in[6];
  const u16* wg = (const u16*)d_in[7];
  const u16* wu = (const u16*)d_in[8];
  const u16* wdn = (const u16*)d_in[9];
  u16* out = (u16*)d_out;
  char* ws = (char*)d_ws;
  u16* abT = (u16*)ws;
  u16* ypart = (u16*)(ws + 5242880);
  float* rsc = (float*)(ws + 10485760);
  float2* stats = (float2*)(ws + 10485760 + 8192);
  unsigned* act = (unsigned*)(ws + 10485760 + 8192 + 16384);
  unsigned* hsel = (unsigned*)(ws + 10485760 + 8192 + 16384 + 24576);
  int* selg = (int*)(ws + 10485760 + 8192 + 16384 + 24576 + 20480);
  float* gsel = (float*)(ws + 10485760 + 8192 + 16384 + 24576 + 20480 + 64);

  k1_ln_gemm<<<256, 512, 0, stream>>>(x, wdre, wdim, abT, wsre, wsim);
  k2_gemm<<<256, 512, 0, stream>>>(wsre, wsim, abT, ypart);
  k3_ln2<<<256, 576, 0, stream>>>(x, ypart, wr, out, rsc, stats, wg, wu, wdn);
  char* wdt = ws + (12u << 20);
  k4_gateup<<<192, 576, 0, stream>>>(x, ypart, rsc, stats, wg, wu, act, hsel, selg, gsel, wdn, wdt);
  k5_down<<<40, 576, 0, stream>>>(wdt, (const u32x4*)act, hsel, selg, gsel, out);
}
```

```cpp
#include <hip/hip_runtime.h>
#include <hip/hip_bf16.h>
#include <stdint.h>

#define K1_PREFETCH 1
#define PF0 6
#define PFS 1
#define SEQ 2048
#define DM 640
#define FF 1536
#define TOPC 8

typedef unsigned short u16;
typedef __attribute__((ext_vector_type(8))) short bf16x8;
typedef __attribute__((ext_vector_type(4))) short s16x4;
typedef __attribute__((ext_vector_type(4))) float f32x4;
typedef __attribute__((ext_vector_type(2))) float f32x2;
typedef __attribute__((ext_vector_type(2))) __bf16 bf16x2_t;
typedef __attribute__((ext_vector_type(4))) unsigned u32x4;
typedef __attribute__((ext_vector_type(2))) unsigned u32x2;

__device__ __forceinline__ unsigned pk2(float a, float b) {
  f32x2 v = {a, b};
  bf16x2_t r = __builtin_convertvector(v, bf16x2_t);
  return __builtin_bit_cast(unsigned, r);
}
__device__ __forceinline__ float lo2f(unsigned w) { return __uint_as_float(w << 16); }
__device__ __forceinline__ float hi2f(unsigned w) { return __uint_as_float(w & 0xffff0000u); }
__device__ __forceinline__ float rbf(float a) { return lo2f(pk2(a, 0.f)); }
__device__ __forceinline__ float bf2f(u16 u) { return __uint_as_float(((unsigned)u) << 16); }

#define WAIT_V(n) asm volatile("s_waitcnt vmcnt(%0)" ::"n"(n) : "memory")
#define WAIT_L(n) asm volatile("s_waitcnt lgkmcnt(%0)" ::"n"(n) : "memory")
#define RAW_BARRIER() do { WAIT_L(0); __builtin_amdgcn_s_barrier(); } while (0)

#define GLDS16(gptr, lptr) __builtin_amdgcn_global_load_lds((const unsigned*)(gptr), (unsigned*)(lptr), 16, 0, 0)

__device__ __forceinline__ void glds16_asm(const void* gsrc, unsigned lds_dst) {
  unsigned keep;
  asm volatile("s_mov_b32 %0, m0\n\ts_mov_b32 m0, %2\n\ts_nop 0\n\tglobal_load_lds_dwordx4 %1, off\n\ts_mov_b32 m0, %0"
               : "=&s"(keep) : "v"(gsrc), "s"(lds_dst) : "memory");
}
__device__ __forceinline__ unsigned lds_addr(const char* p) {
  return (unsigned)(uintptr_t)(const __attribute__((address_space(3))) char*)p;
}

__device__ __forceinline__ unsigned wave_max_u32(unsigned v) {
  v = max(v, (unsigned)__builtin_amdgcn_update_dpp(0, (int)v, 0x111, 0xf, 0xf, false));
  v = max(v, (unsigned)__builtin_amdgcn_update_dpp(0, (int)v, 0x112, 0xf, 0xf, false));
  v = max(v, (unsigned)__builtin_amdgcn_update_dpp(0, (int)v, 0x114, 0xf, 0xf, false));
  v = max(v, (unsigned)__builtin_amdgcn_update_dpp(0, (int)v, 0x118, 0xf, 0xf, false));
  v = max(v, (unsigned)__builtin_amdgcn_update_dpp(0, (int)v, 0x142, 0xa, 0xf, false));
  v = max(v, (unsigned)__builtin_amdgcn_update_dpp(0, (int)v, 0x143, 0xc, 0xf, false));
  return (unsigned)__builtin_amdgcn_readlane((int)v, 63);
}
__device__ __forceinline__ float dpp_f(float v, int ctrl_dummy);
template <int CTRL, int RM>
__device__ __forceinline__ float dppf(float v) {
  return __int_as_float(__builtin_amdgcn_update_dpp(0, __float_as_int(v), CTRL, RM, 0xf, false));
}
__device__ __forceinline__ float wave_sum_f32(float v) {
  v += dppf<0x111, 0xf>(v);
  v += dppf<0x112, 0xf>(v);
  v += dppf<0x114, 0xf>(v);
  v += dppf<0x118, 0xf>(v);
  v += dppf<0x142, 0xa>(v);
  v += dppf<0x143, 0xc>(v);
  return __int_as_float(__builtin_amdgcn_readlane(__float_as_int(v), 63));
}

#define K1_AOFF 0
#define K1_BOFF 81920
#define K1_BST 20480
#define K1_DUMMY (81920 + 3 * 20480)
#define K1_TLD 144
__device__ __forceinline__ float red8(float v) {
  v += dppf<0xB1, 0xf>(v);
  v += dppf<0x4E, 0xf>(v);
  v += dppf<0x141, 0xf>(v);
  return v;
}
__device__ __forceinline__ void k1_ln_rows(const u32x4 (&a)[10], char* atile, int row, int c8) {
  f32x2 v[40];
  f32x2 s2 = {0.f, 0.f};
#pragma unroll
  for (int jj = 0; jj < 10; ++jj)
#pragma unroll
    for (int c = 0; c < 4; ++c) {
      v[jj * 4 + c] = (f32x2){lo2f(a[jj][c]), hi2f(a[jj][c])};
      s2 += v[jj * 4 + c];
    }
  const float s = red8(s2[0] + s2[1]);
  const float mean = s * (1.0f / DM);
  const float mb = rbf(mean);
  const f32x2 mb2 = {mb, mb};
  f32x2 q2 = {0.f, 0.f};
#pragma unroll
  for (int i = 0; i < 40; ++i) {
    const f32x2 d = v[i] - mb2;
    q2 = __builtin_elementwise_fma(d, d, q2);
    const unsigned u = pk2(d[0], d[1]);
    v[i] = (f32x2){lo2f(u), hi2f(u)};
  }
  const float ss = red8(q2[0] + q2[1]);
  const float dm = mean - mb;
  const float var = fmaxf(ss * (1.0f / DM) - dm * dm, 0.f);
  const float vb = rbf(var);
  const float sd = rbf(sqrtf(rbf(vb + 1e-5f)));
  const float rs = 1.0f / sd;
  const f32x2 rs2 = {rs, rs};
  char* ap = atile + row * 128 + ((c8 ^ ((row >> 1) & 7)) << 4);
#pragma unroll
  for (int jj = 0; jj < 10; ++jj) {
    u32x4 o;
#pragma unroll
    for (int c = 0; c < 4; ++c) {
      const f32x2 n = v[jj * 4 + c] * rs2;
      o[c] = pk2(n[0], n[1]);
    }
    *(u32x4*)(ap + jj * 8192) = o;
  }
}
__global__ __launch_bounds__(512) void k1_ln_gemm(const u16* __restrict__ x, const u16* __restrict__ wre,
                                                   const u16* __restrict__ wim, u16* __restrict__ abT,
                                                   const u16* __restrict__ wsre, const u16* __restrict__ wsim) {
  __shared__ __attribute__((aligned(1024))) char smem[K1_DUMMY + 1024];
  const int tid = threadIdx.x, lane = tid & 63, q = lane >> 4, r = lane & 15;
  const int w = __builtin_amdgcn_readfirstlane(tid >> 6);
  const int xcd = blockIdx.x & 7, j = blockIdx.x >> 3;
  const int mt = (xcd >> 1) * 8 + (j & 7);
  const int nt = (xcd & 1) * 4 + (j >> 3);
  const int s0 = mt * 64;

  const int row8 = lane >> 3, c8 = lane & 7;
  const u16* W = (nt < 4) ? (wre + (size_t)nt * 160 * DM) : (wim + (size_t)(nt - 4) * 160 * DM);
  const int lw = w - 4;
  const char* pfp = (const char*)((xcd >> 2) ? wsim : wsre) + (size_t)(xcd & 3) * 512 * SEQ * 2 + (size_t)j * 65536 + lane * 16;
  const u16* xr = x + (size_t)(s0 + w * 8 + row8) * DM + c8 * 8;
#define K1_XLOAD()                                                                                  \
  _Pragma("unroll") for (int jj = 0; jj < 10; ++jj)                                                 \
    asm volatile("global_load_dwordx4 %0, %1, off offset:%2" : "=v"(a[jj]) : "v"(xr), "n"(jj * 128) : "memory")
#define K1_XWAIT(n)                                                                                 \
  asm volatile("s_waitcnt vmcnt(" #n ")"                                                            \
               : "+v"(a[0]), "+v"(a[1]), "+v"(a[2]), "+v"(a[3]), "+v"(a[4]), "+v"(a[5]), "+v"(a[6]), "+v"(a[7]), "+v"(a[8]), "+v"(a[9]) \
               :: "memory")
  if (w >= 4) {
    u32x4 a[10];
    K1_XLOAD();
    int boff[5];
#pragma unroll
    for (int i = 0; i < 5; ++i) {
      const int row = (lw * 5 + i) * 8 + row8;
      boff[i] = row * DM + ((c8 ^ ((row >> 1) & 7)) << 3);
    }
#define K1_ISSUE(stage, buf)                                                                        \
  do {                                                                                              \
    _Pragma("unroll") for (int i = 0; i < 5; ++i)                                                   \
      glds16_asm(W + boff[i] + (stage) * 64, lds_addr(smem) + K1_BOFF + (buf) * K1_BST + (lw * 5 + i) * 1024); \
  } while (0)
    K1_ISSUE(0, 0);
    K1_ISSUE(1, 1);
    K1_ISSUE(2, 2);
    K1_XWAIT(15);
    k1_ln_rows(a, smem + K1_AOFF, w * 8 + row8, c8);
    WAIT_V(10);
    WAIT_L(0);
    __builtin_amdgcn_s_barrier();
#pragma unroll
    for (int st = 0; st < 10; ++st) {
      if (st < 8) WAIT_V(5); else WAIT_V(0);
      __builtin_amdgcn_s_barrier();
      if (st + 3 < 10) K1_ISSUE(st + 3, st % 3);
    }
  } else {
    u32x4 a[10];
    K1_XLOAD();
    K1_XWAIT(0);
    k1_ln_rows(a, smem + K1_AOFF, w * 8 + row8, c8);
#pragma unroll
    for (int i = 0; i < PF0; ++i) glds16_asm(pfp + (w * 16 + i) * 1024, lds_addr(smem) + K1_DUMMY);
    const int wm = w >> 1, wn = w & 1;
    f32x4 acc[2][5];
#pragma unroll
    for (int m = 0; m < 2; ++m)
#pragma unroll
      for (int n = 0; n < 5; ++n) acc[m][n] = (f32x4){0.f, 0.f, 0.f, 0.f};
    const int sw = (r >> 1) & 7;
    const int aro = K1_AOFF + (wm * 32 + r) * 128;
    const int bro = K1_BOFF + (wn * 80 + r) * 128;
    const int co0 = ((0 * 4 + q) ^ sw) << 4, co1 = ((1 * 4 + q) ^ sw) << 4;
    bf16x8 xa[2], xb[5], ya[2], yb[5];
#define K1_READ(fa, fb, st, h)                                                                      \
  do {                                                                                              \
    const int co_ = (h) ? co1 : co0;                                                                \
    _Pragma("unroll") for (int n = 0; n < 5; ++n)                                                   \
      fb[n] = *(const bf16x8*)(smem + bro + ((st) % 3) * K1_BST + n * 2048 + co_);                  \
    _Pragma("unroll") for (int m = 0; m < 2; ++m)                                                   \
      fa[m] = *(const bf16x8*)(smem + aro + (st) * 8192 + m * 2048 + co_);                          \
  } while (0)
#define K1_MMA(fa, fb)                                                                              \
  do {                                                                                              \
    _Pragma("unroll") for (int m = 0; m < 2; ++m)                                                   \
      _Pragma("unroll") for (int n = 0; n < 5; ++n)                                                 \
        acc[m][n] = __builtin_amdgcn_mfma_f32_16x16x32_bf16(fa[m], fb[n], acc[m][n], 0, 0, 0);      \
  } while (0)
#define SB() __builtin_amdgcn_sched_barrier(0)
    WAIT_L(0);
    __builtin_amdgcn_s_barrier();
    K1_READ(xa, xb, 0, 0);
#pragma unroll
    for (int st = 0; st < 10; ++st) {
      SB(); K1_READ(ya, yb, st, 1); SB(); K1_MMA(xa, xb);
      SB(); WAIT_L(0); __builtin_amdgcn_s_barrier(); SB();
      if (st < 9) K1_READ(xa, xb, st + 1, 0);
      _Pragma("unroll") for (int i = PF0 + st * PFS; i < PF0 + (st + 1) * PFS; ++i) if (i < 16) glds16_asm(pfp + (w * 16 + i) * 1024, lds_addr(smem) + K1_DUMMY);
      SB(); K1_MMA(ya, yb);
    }
#pragma unroll
    for (int m = 0; m < 2; ++m)
#pragma unroll
      for (int n = 0; n < 5; ++n) {
        u32x2 o;
        o[0] = pk2(acc[m][n][0], acc[m][n][1]);
        o[1] = pk2(acc[m][n][2], acc[m][n][3]);
        *(u32x2*)(smem + (wn * 80 + n * 16 + r) * K1_TLD + (wm * 32 + m * 16 + q * 4) * 2) = o;
      }
  }
  WAIT_L(0);
  __builtin_amdgcn_s_barrier();
#pragma unroll
  for (int i = 0; i < 3; ++i) {
    const int c = tid + i * 512;
    if (c < 1280) {
      const int e = c >> 3, p = c & 7;
      const u32x4 o = *(const u32x4*)(smem + e * K1_TLD + p * 16);
      *(u32x4*)(abT + (size_t)(nt * 160 + e) * SEQ + s0 + p * 8) = o;
    }
  }
  WAIT_V(0);
}

#define K2_STAGE 53248
#define K2_TLD 176
__global__ __launch_bounds__(512) void k2_gemm(const u16* __restrict__ wsre, const u16* __restrict__ wsim,
                                                const u16* __restrict__ abT, u16* __restrict__ ypart) {
  __shared__ __attribute__((aligned(1024))) char smem[3 * K2_STAGE];
  const int tid = threadIdx.x, lane = tid & 63, q = lane >> 4, r = lane & 15;
  const int w = __builtin_amdgcn_readfirstlane(tid >> 6);
  const int xcd = blockIdx.x & 7, j = blockIdx.x >> 3;
  const int part = xcd >> 2;
  const int mt = (xcd & 3) * 4 + (j & 3);
  const int nt = j >> 2;
  const int t0 = mt * 128, e0 = nt * 80;

  if (w >= 4) {
    const int lw = w - 4;
    const u16* A = (part ? wsim : wsre) + (size_t)t0 * SEQ;
    const u16* B = abT + (size_t)(part * DM + e0) * SEQ;
    const int rin = lane >> 3, pos = lane & 7;
    int aoff[4], boff[5], bl[5];
#pragma unroll
    for (int p = 0; p < 4; ++p) {
      const int row = lw * 32 + p * 8 + rin;
      aoff[p] = row * SEQ + ((pos ^ ((row >> 1) & 7)) << 3);
    }
#pragma unroll
    for (int i = 0; i < 5; ++i) {
      const int pc = lw * 5 + i;
      const int sub = pc / 10, p = pc % 10;
      const int row = p * 8 + rin;
      boff[i] = row * SEQ + sub * 64 + ((pos ^ ((row >> 1) & 7)) << 3);
      bl[i] = 32768 + sub * 10240 + p * 1024;
    }
#define K2_ISSUE(stage, buf)                                                           \
  do {                                                                                 \
    char* sb_ = smem + (buf) * K2_STAGE;                                               \
    const int kb_ = (stage) * 128;                                                     \
    _Pragma("unroll") for (int sub = 0; sub < 2; ++sub)                                \
      _Pragma("unroll") for (int p = 0; p < 4; ++p)                                    \
        GLDS16(A + aoff[p] + kb_ + sub * 64, sb_ + sub * 16384 + (lw * 4 + p) * 1024); \
    _Pragma("unroll") for (int i = 0; i < 5; ++i)                                      \
      GLDS16(B + boff[i] + kb_, sb_ + bl[i]);                                          \
  } while (0)
    K2_ISSUE(0, 0);
    K2_ISSUE(1, 1);
    K2_ISSUE(2, 2);
    WAIT_V(26);
    __builtin_amdgcn_s_barrier();
    int buf = 0;
    for (int st = 0; st < 16; ++st) {
      if (st < 14) WAIT_V(13); else WAIT_V(0);
      __builtin_amdgcn_s_barrier();
      if (st + 3 < 16) K2_ISSUE(st + 3, buf);
      buf = buf + 1; if (buf >= 3) buf = 0;
    }
  } else {
    f32x4 acc[2][5];
#pragma unroll
    for (int m = 0; m < 2; ++m)
#pragma unroll
      for (int n = 0; n < 5; ++n) acc[m][n] = (f32x4){0.f, 0.f, 0.f, 0.f};
    const int sw = (r >> 1) & 7;
    const int aro = (w * 32 + r) * 128;
    const int bro = 32768 + r * 128;
    const int co0 = ((0 * 4 + q) ^ sw) << 4, co1 = ((1 * 4 + q) ^ sw) << 4;
    bf16x8 xa[2], xb[5], ya[2], yb[5];
#define K2_READ(fa, fb, sbp, s)                                                                              \
  do {                                                                                                       \
    const int co_ = ((s) & 1) ? co1 : co0;                                                                   \
    _Pragma("unroll") for (int n = 0; n < 5; ++n)                                                            \
      fb[n] = *(const bf16x8*)((sbp) + bro + ((s) >> 1) * 10240 + n * 2048 + co_);                           \
    _Pragma("unroll") for (int m = 0; m < 2; ++m)                                                            \
      fa[m] = *(const bf16x8*)((sbp) + aro + ((s) >> 1) * 16384 + m * 2048 + co_);                           \
  } while (0)
#define K2_MMA(fa, fb)                                                                                       \
  do {                                                                                                       \
    _Pragma("unroll") for (int m = 0; m < 2; ++m)                                                            \
      _Pragma("unroll") for (int n = 0; n < 5; ++n)                                                          \
        acc[m][n] = __builtin_amdgcn_mfma_f32_16x16x32_bf16(fb[n], fa[m], acc[m][n], 0, 0, 0);               \
  } while (0)
    __builtin_amdgcn_s_barrier();
    K2_READ(xa, xb, smem, 0);
    int buf = 0;
    for (int st = 0; st < 16; ++st) {
      const char* sb = smem + buf * K2_STAGE;
      int nb = buf + 1; if (nb >= 3) nb = 0;
      const char* sn = smem + nb * K2_STAGE;
      SB(); K2_READ(ya, yb, sb, 1); SB(); K2_MMA(xa, xb);
      SB(); K2_READ(xa, xb, sb, 2); SB(); K2_MMA(ya, yb);
      SB(); K2_READ(ya, yb, sb, 3); SB(); K2_MMA(xa, xb);
      SB(); WAIT_L(0); __builtin_amdgcn_s_barrier(); SB();
      if (st < 15) K2_READ(xa, xb, sn, 0);
      SB(); K2_MMA(ya, yb);
      buf = nb;
    }
#pragma unroll
    for (int m = 0; m < 2; ++m)
#pragma unroll
      for (int n = 0; n < 5; ++n) {
        u32x2 o;
        o[0] = pk2(acc[m][n][0], acc[m][n][1]);
        o[1] = pk2(acc[m][n][2], acc[m][n][3]);
        *(u32x2*)(smem + (w * 32 + m * 16 + r) * K2_TLD + (n * 16 + q * 4) * 2) = o;
      }
  }
  WAIT_L(0);
  __builtin_amdgcn_s_barrier();
  {
    u16* Y = ypart + (size_t)part * SEQ * DM;
#pragma unroll
    for (int i = 0; i < 3; ++i) {
      const int c = tid + i * 512;
      if (c < 1280) {
        const int t = c / 10, p = c % 10;
        const u32x4 o = *(const u32x4*)(smem + t * K2_TLD + p * 16);
        *(u32x4*)(Y + (size_t)(t0 + t) * DM + e0 + p * 8) = o;
      }
    }
  }
}

__global__ __launch_bounds__(576) void k3_ln2(const u16* __restrict__ x, const u16* __restrict__ ypart,
                                               const u16* __restrict__ wr, u16* __restrict__ out,
                                               float* __restrict__ rsc, float2* __restrict__ stats,
                                               const u16* __restrict__ wg, const u16* __restrict__ wu,
                                               const u16* __restrict__ wdn) {
  const int lane = threadIdx.x & 63;
  if (__builtin_amdgcn_readfirstlane(threadIdx.x >> 6) == 8) {
    __shared__ __attribute__((aligned(1024))) char dl[1024];
    const size_t fo = (size_t)blockIdx.x * 7680 + lane * 16;
#pragma unroll
    for (int i = 0; i < 7; ++i) {
      GLDS16((const char*)wg + fo + i * 1024, dl);
      GLDS16((const char*)wu + fo + i * 1024, dl);
    }
    if (lane < 32) {
      GLDS16((const char*)wg + fo + 7168, dl);
      GLDS16((const char*)wu + fo + 7168, dl);
    }
    WAIT_V(0);
    return;
  }
  const int row = blockIdx.x * 8 + (threadIdx.x >> 6);
  const unsigned* xp = (const unsigned*)(x + (size_t)row * DM);
  const unsigned* yr = (const unsigned*)(ypart + (size_t)row * DM);
  const unsigned* yi = (const unsigned*)(ypart + (size_t)SEQ * DM + (size_t)row * DM);
  const unsigned* wp = (const unsigned*)wr;
  unsigned xv[5], rv[5], iv[5], wv[5];
#pragma unroll
  for (int i = 0; i < 5; ++i) {
    xv[i] = xp[i * 64 + lane];
    rv[i] = yr[i * 64 + lane];
    iv[i] = yi[i * 64 + lane];
    wv[i] = wp[i * 64 + lane];
  }
  float h[10];
  float s = 0.f;
#pragma unroll
  for (int i = 0; i < 5; ++i) {
    const float y0 = rbf(lo2f(rv[i]) - lo2f(iv[i])), y1 = rbf(hi2f(rv[i]) - hi2f(iv[i]));
    h[2 * i] = rbf(lo2f(xv[i]) + y0);
    h[2 * i + 1] = rbf(hi2f(xv[i]) + y1);
    s += h[2 * i] + h[2 * i + 1];
  }
  s = wave_sum_f32(s);
  const float mean = s * (1.0f / DM);
  const float mb = rbf(mean);
  float d[10];
  float ss = 0.f;
#pragma unroll
  for (int i = 0; i < 10; ++i) {
    d[i] = h[i] - mb;
    ss += d[i] * d[i];
  }
  ss = wave_sum_f32(ss);
  const float dm = mean - mb;
  float var = fmaxf(ss * (1.0f / DM) - dm * dm, 0.f);
  const float vb = rbf(var);
  const float sd = rbf(sqrtf(rbf(vb + 1e-5f)));
  const float rs = 1.0f / sd;
  float dot = 0.f;
  unsigned* op = (unsigned*)(out + (size_t)row * DM);
#pragma unroll
  for (int i = 0; i < 5; ++i) {
    const float n0 = rbf(rbf(d[2 * i]) * rs), n1 = rbf(rbf(d[2 * i + 1]) * rs);
    dot += n0 * lo2f(wv[i]);
    dot += n1 * hi2f(wv[i]);
    op[i * 64 + lane] = pk2(h[2 * i] + n0, h[2 * i + 1] + n1);
  }
  dot = wave_sum_f32(dot);
  if (lane == 0) {
    rsc[row] = rbf(dot);
    stats[row] = make_float2(mb, rs);
  }
}

__device__ __forceinline__ bf16x8 ldtr_frag(const char* tile, int k0, int lane) {
  const int g = lane >> 4, i = lane & 15;
  const char* p = tile + (k0 + 8 * g + (i >> 2)) * 32 + (i & 3) * 8;
  s16x4 lo = __builtin_amdgcn_ds_read_tr16_b64_v4i16((__attribute__((address_space(3))) s16x4*)(p));
  s16x4 hi = __builtin_amdgcn_ds_read_tr16_b64_v4i16((__attribute__((address_space(3))) s16x4*)(p + 128));
  bf16x8 f;
  f[0] = lo[0]; f[1] = lo[1]; f[2] = lo[2]; f[3] = lo[3];
  f[4] = hi[0]; f[5] = hi[1]; f[6] = hi[2]; f[7] = hi[3];
  return f;
}

#define XS_LD 1296
#define K4_XS 40960
#define K4_RED (40960 + 8 * XS_LD)
#define K4_CAND (K4_RED + 4096)
__global__ __launch_bounds__(576) void k4_gateup(const u16* __restrict__ x, const u16* __restrict__ ypart,
                                                  const float* __restrict__ rsc, const float2* __restrict__ stats,
                                                  const u16* __restrict__ wg, const u16* __restrict__ wu,
                                                  unsigned* __restrict__ act, unsigned* __restrict__ hsel,
                                                  int* __restrict__ selg, float* __restrict__ gsel,
                                                  const u16* __restrict__ wd, char* __restrict__ wdt) {
  __shared__ __attribute__((aligned(1024))) char smem[K4_CAND + 256];
  const int tid = threadIdx.x, lane = tid & 63, q = lane >> 4, r = lane & 15;
  const int w = __builtin_amdgcn_readfirstlane(tid >> 6);
  if (blockIdx.x >= 96) {
    if (w == 8) return;
    const int b2 = blockIdx.x - 96;
    const char* srcp = (const char*)wd + (size_t)b2 * 16 * 1280;
    u32x4 v[3];
#pragma unroll
    for (int i = 0; i < 3; ++i) { const int c = i * 512 + tid; if (c < 1280) v[i] = __builtin_nontemporal_load((const u32x4*)(srcp + c * 16)); }
#pragma unroll
    for (int i = 0; i < 3; ++i) { const int c = i * 512 + tid; if (c < 1280) *(u32x4*)(smem + (c / 80) * 1296 + (c % 80) * 16) = v[i]; }
    WAIT_L(0);
    __builtin_amdgcn_s_barrier();
#pragma unroll
    for (int i = 0; i < 3; ++i) {
      const int d = i * 512 + tid;
      if (d < 1280) {
        const int cg = d >> 5, rw = (d & 31) >> 1, hf = d & 1;
        const u32x4 o = *(const u32x4*)(smem + rw * 1296 + cg * 32 + hf * 16);
        *(u32x4*)(wdt + (size_t)cg * 49152 + (b2 * 16 + rw) * 32 + hf * 16) = o;
      }
    }
    return;
  }
  const int xcd = blockIdx.x & 7, jj = blockIdx.x >> 3;
  const int f0 = ((jj >> 2) * 32 + xcd * 4 + (jj & 3)) * 16;
  if (w == 8) {
#pragma unroll 4
    for (int p = 0; p < 20; ++p) {
      const int row = p * 32 + (lane >> 1);
      GLDS16(wg + (size_t)row * FF + f0 + (lane & 1) * 8, smem + p * 1024);
      GLDS16(wu + (size_t)row * FF + f0 + (lane & 1) * 8, smem + 20480 + p * 1024);
    }
    __builtin_amdgcn_s_barrier();
    WAIT_V(0);
    return;
  }
  unsigned key[4];
  {
    const u32x4 v = *(const u32x4*)(rsc + tid * 4);
#pragma unroll
    for (int i = 0; i < 4; ++i) {
      const unsigned u = v[i];
      const unsigned o = (u & 0x80000000u) ? ~u : (u | 0x80000000u);
      key[i] = (o & 0xffff0000u) | (0xffffu - (unsigned)(tid * 4 + i));
    }
  }
  unsigned* cand = (unsigned*)(smem + K4_CAND);
#pragma unroll
  for (int rnd = 0; rnd < 8; ++rnd) {
    const unsigned lm = max(max(key[0], key[1]), max(key[2], key[3]));
    const unsigned wm = wave_max_u32(lm);
#pragma unroll
    for (int i = 0; i < 4; ++i) key[i] = (key[i] == wm) ? 0u : key[i];
    if (lane == 0) cand[w * 8 + rnd] = wm;
  }
  WAIT_L(0);
  __builtin_amdgcn_s_barrier();
  unsigned myk = 0;
  {
    unsigned k2 = cand[lane];
#pragma unroll
    for (int rnd = 0; rnd < 8; ++rnd) {
      const unsigned wm = wave_max_u32(k2);
      k2 = (k2 == wm) ? 0u : k2;
      myk = (w == rnd) ? wm : myk;
    }
  }
  {
    const int row = (int)(0xffffu - (myk & 0xffffu));
    const float2 st = stats[row];
    const unsigned* xp = (const unsigned*)(x + (size_t)row * DM);
    const unsigned* yr = (const unsigned*)(ypart + (size_t)row * DM);
    const unsigned* yi = (const unsigned*)(ypart + (size_t)SEQ * DM + (size_t)row * DM);
    unsigned xv[5], rv[5], iv[5];
#pragma unroll
    for (int i = 0; i < 5; ++i) { xv[i] = xp[i * 64 + lane]; rv[i] = yr[i * 64 + lane]; iv[i] = yi[i * 64 + lane]; }
#pragma unroll
    for (int i = 0; i < 5; ++i) {
      const float h0 = rbf(lo2f(xv[i]) + rbf(lo2f(rv[i]) - lo2f(iv[i])));
      const float h1 = rbf(hi2f(xv[i]) + rbf(hi2f(rv[i]) - hi2f(iv[i])));
      const unsigned hn = pk2(rbf(h0 - st.x) * st.y, rbf(h1 - st.x) * st.y);
      *(unsigned*)(smem + K4_XS + w * XS_LD + (i * 64 + lane) * 4) = hn;
      if (blockIdx.x == 0) {
        hsel[w * 320 + i * 64 + lane] = pk2(h0, h1);
        hsel[2560 + w * 320 + i * 64 + lane] = hn;
      }
    }
    if (blockIdx.x == 0 && lane == 0) {
      const unsigned ob = myk & 0xffff0000u;
      const float rr = __uint_as_float((ob & 0x80000000u) ? (ob & 0x7fffffffu) : (~ob & 0xffff0000u));
      selg[w] = row;
      gsel[w] = rbf(1.0f / (1.0f + __expf(-rr)));
    }
  }
  WAIT_L(0);
  __builtin_amdgcn_s_barrier();
  float* red = (float*)(smem + K4_RED);
  {
    const char* tile = smem + (w >> 2) * 20480;
    f32x4 acc = {0.f, 0.f, 0.f, 0.f};
#pragma unroll
    for (int ks = 0; ks < 5; ++ks) {
      const int k0 = (w & 3) * 160 + ks * 32;
      bf16x8 af = *(const bf16x8*)(smem + K4_XS + (r & 7) * XS_LD + (k0 + q * 8) * 2);
      if (r >= 8) af = (bf16x8){0, 0, 0, 0, 0, 0, 0, 0};
      const bf16x8 bw = ldtr_frag(tile, k0, lane);
      acc = __builtin_amdgcn_mfma_f32_16x16x32_bf16(af, bw, acc, 0, 0, 0);
    }
    if (lane < 32) *(f32x4*)(red + w * 128 + lane * 4) = acc;
  }
  WAIT_L(0);
  __builtin_amdgcn_s_barrier();
  if (tid < 64) {
    const int tk = tid >> 3, pr = tid & 7;
    float av[2];
#pragma unroll
    for (int e = 0; e < 2; ++e) {
      const int li = ((tk >> 2) * 16 + pr * 2 + e) * 4 + (tk & 3);
      float g = 0.f, u = 0.f;
#pragma unroll
      for (int ww = 0; ww < 4; ++ww) {
        g += red[ww * 128 + li];
        u += red[(ww + 4) * 128 + li];
      }
      g = rbf(g);
      u = rbf(u);
      const float sg = rbf(g / (1.0f + __expf(-g)));
      av[e] = sg * u;
    }
    act[tk * 768 + (f0 >> 1) + pr] = pk2(av[0], av[1]);
  }
}

#define AC_LD 3088
#define K5_AS 49152
#define K5_RED (49152 + 8 * AC_LD)
__global__ __launch_bounds__(576) void k5_down(const char* __restrict__ wdt, const u32x4* __restrict__ act,
                                                const unsigned* __restrict__ hsel, const int* __restrict__ selg,
                                                const float* __restrict__ gsel, u16* __restrict__ out) {
  __shared__ __attribute__((aligned(1024))) char smem[K5_RED + 4096];
  const int tid = threadIdx.x, lane = tid & 63, q = lane >> 4, r = lane & 15;
  const int w = __builtin_amdgcn_readfirstlane(tid >> 6);
  const int xcd = blockIdx.x & 7, jj = blockIdx.x >> 3;
  const int c0 = (xcd * 5 + jj) * 16;
  if (w == 8) {
#pragma unroll 4
    for (int p = 0; p < 48; ++p) GLDS16(wdt + (size_t)(c0 >> 4) * 49152 + p * 1024 + lane * 16, smem + p * 1024);
    WAIT_V(0);
    return;
  }
  u32x4 av[3];
#pragma unroll
  for (int i = 0; i < 3; ++i) av[i] = act[i * 512 + tid];
  const int tk = (tid >> 3) & 7, pr = tid & 7;
  const unsigned hv = hsel[tk * 320 + (c0 >> 1) + pr];
  const unsigned nv = hsel[2560 + tk * 320 + (c0 >> 1) + pr];
  const int orow = selg[tk];
  const float sgm = gsel[tk];
#pragma unroll
  for (int i = 0; i < 3; ++i) {
    const int idx = i * 512 + tid;
    *(u32x4*)(smem + K5_AS + (idx / 192) * AC_LD + (idx % 192) * 16) = av[i];
  }
  WAIT_L(0);
  __builtin_amdgcn_s_barrier();
  float* red = (float*)(smem + K5_RED);
  {
    f32x4 acc = {0.f, 0.f, 0.f, 0.f};
#pragma unroll
    for (int ks = 0; ks < 6; ++ks) {
      const int k0 = w * 192 + ks * 32;
      bf16x8 af = *(const bf16x8*)(smem + K5_AS + (r & 7) * AC_LD + (k0 + q * 8) * 2);
      if (r >= 8) af = (bf16x8){0, 0, 0, 0, 0, 0, 0, 0};
      const bf16x8 bw = ldtr_frag(smem, k0, lane);
      acc = __builtin_amdgcn_mfma_f32_16x16x32_bf16(af, bw, acc, 0, 0, 0);
    }
    if (lane < 32) *(f32x4*)(red + w * 128 + lane * 4) = acc;
  }
  WAIT_L(0);
  __builtin_amdgcn_s_barrier();
  if (tid < 64) {
    float ov[2];
#pragma unroll
    for (int e = 0; e < 2; ++e) {
      const int li = ((tk >> 2) * 16 + pr * 2 + e) * 4 + (tk & 3);
      float y = 0.f;
#pragma unroll
      for (int ww = 0; ww < 8; ++ww) y += red[ww * 128 + li];
      y = rbf(rbf(y) * sgm);
      const float h = e ? hi2f(hv) : lo2f(hv), hn = e ? hi2f(nv) : lo2f(nv);
      ov[e] = h + rbf(hn + y);
    }
    *(unsigned*)(out + (size_t)orow * DM + c0 + pr * 2) = pk2(ov[0], ov[1]);
  }
}

extern "C" void kernel_launch(void* const* d_in, const int* in_sizes, int n_in, void* d_out, int out_size,
                              void* d_ws, size_t ws_size, hipStream_t stream) {
  const u16* x = (const u16*)d_in[0];
  const u16* wsre = (const u16*)d_in[1];
  const u16* wsim = (const u16*)d_in[2];
  const u16* wdre = (const u16*)d_in[3];
  const u16* wdim = (const u16*)d_in[4];
  const u16* wr = (const u16*)d_in[6];
  const u16* wg = (const u16*)d_in[7];
  const u16* wu = (const u16*)d_in[8];
  const u16* wdn = (const u16*)d_in[9];
  u16* out = (u16*)d_out;
  char* ws = (char*)d_ws;
  u16* abT = (u16*)ws;
  u16* ypart = (u16*)(ws + 5242880);
  float* rsc = (float*)(ws + 10485760);
  float2* stats = (float2*)(ws + 10485760 + 8192);
  unsigned* act = (unsigned*)(ws + 10485760 + 8192 + 16384);
  unsigned* hsel = (unsigned*)(ws + 10485760 + 8192 + 16384 + 24576);
  int* selg = (int*)(ws + 10485760 + 8192 + 16384 + 24576 + 20480);
  float* gsel = (float*)(ws + 10485760 + 8192 + 16384 + 24576 + 20480 + 64);

  k1_ln_gemm<<<256, 512, 0, stream>>>(x, wdre, wdim, abT, wsre, wsim);
  k2_gemm<<<256, 512, 0, stream>>>(wsre, wsim, abT, ypart);
  k3_ln2<<<256, 576, 0, stream>>>(x, ypart, wr, out, rsc, stats, wg, wu, wdn);
  char* wdt = ws + (12u << 20);
  k4_gateup<<<192, 576, 0, stream>>>(x, ypart, rsc, stats, wg, wu, act, hsel, selg, gsel, wdn, wdt);
  k5_down<<<40, 576, 0, stream>>>(wdt, (const u32x4*)act, hsel, selg, gsel, out);
}
```

```cpp
#include <hip/hip_runtime.h>
#include <hip/hip_bf16.h>
#include <stdint.h>

#define K1_PREFETCH 1
#define SEQ 2048
#define DM 640
#define FF 1536
#define TOPC 8

typedef unsigned short u16;
typedef __attribute__((ext_vector_type(8))) short bf16x8;
typedef __attribute__((ext_vector_type(4))) short s16x4;
typedef __attribute__((ext_vector_type(4))) float f32x4;
typedef __attribute__((ext_vector_type(2))) float f32x2;
typedef __attribute__((ext_vector_type(2))) __bf16 bf16x2_t;
typedef __attribute__((ext_vector_type(4))) unsigned u32x4;
typedef __attribute__((ext_vector_type(2))) unsigned u32x2;

__device__ __forceinline__ unsigned pk2(float a, float b) {
  f32x2 v = {a, b};
  bf16x2_t r = __builtin_convertvector(v, bf16x2_t);
  return __builtin_bit_cast(unsigned, r);
}
__device__ __forceinline__ float lo2f(unsigned w) { return __uint_as_float(w << 16); }
__device__ __forceinline__ float hi2f(unsigned w) { return __uint_as_float(w & 0xffff0000u); }
__device__ __forceinline__ float rbf(float a) { return lo2f(pk2(a, 0.f)); }
__device__ __forceinline__ float bf2f(u16 u) { return __uint_as_float(((unsigned)u) << 16); }

#define WAIT_V(n) asm volatile("s_waitcnt vmcnt(%0)" ::"n"(n) : "memory")
#define WAIT_L(n) asm volatile("s_waitcnt lgkmcnt(%0)" ::"n"(n) : "memory")
#define RAW_BARRIER() do { WAIT_L(0); __builtin_amdgcn_s_barrier(); } while (0)

#define GLDS16(gptr, lptr) __builtin_amdgcn_global_load_lds((const unsigned*)(gptr), (unsigned*)(lptr), 16, 0, 0)

__device__ __forceinline__ void glds16_asm(const void* gsrc, unsigned lds_dst) {
  unsigned keep;
  asm volatile("s_mov_b32 %0, m0\n\ts_mov_b32 m0, %2\n\ts_nop 0\n\tglobal_load_lds_dwordx4 %1, off\n\ts_mov_b32 m0, %0"
               : "=&s"(keep) : "v"(gsrc), "s"(lds_dst) : "memory");
}
__device__ __forceinline__ unsigned lds_addr(const char* p) {
  return (unsigned)(uintptr_t)(const __attribute__((address_space(3))) char*)p;
}

__device__ __forceinline__ unsigned wave_max_u32(unsigned v) {
  v = max(v, (unsigned)__builtin_amdgcn_update_dpp(0, (int)v, 0x111, 0xf, 0xf, false));
  v = max(v, (unsigned)__builtin_amdgcn_update_dpp(0, (int)v, 0x112, 0xf, 0xf, false));
  v = max(v, (unsigned)__builtin_amdgcn_update_dpp(0, (int)v, 0x114, 0xf, 0xf, false));
  v = max(v, (unsigned)__builtin_amdgcn_update_dpp(0, (int)v, 0x118, 0xf, 0xf, false));
  v = max(v, (unsigned)__builtin_amdgcn_update_dpp(0, (int)v, 0x142, 0xa, 0xf, false));
  v = max(v, (unsigned)__builtin_amdgcn_update_dpp(0, (int)v, 0x143, 0xc, 0xf, false));
  return (unsigned)__builtin_amdgcn_readlane((int)v, 63);
}
__device__ __forceinline__ float dpp_f(float v, int ctrl_dummy);
template <int CTRL, int RM>
__device__ __forceinline__ float dppf(float v) {
  return __int_as_float(__builtin_amdgcn_update_dpp(0, __float_as_int(v), CTRL, RM, 0xf, false));
}
__device__ __forceinline__ float wave_sum_f32(float v) {
  v += dppf<0x111, 0xf>(v);
  v += dppf<0x112, 0xf>(v);
  v += dppf<0x114, 0xf>(v);
  v += dppf<0x118, 0xf>(v);
  v += dppf<0x142, 0xa>(v);
  v += dppf<0x143, 0xc>(v);
  return __int_as_float(__builtin_amdgcn_readlane(__float_as_int(v), 63));
}

#define K1_AOFF 0
#define K1_BOFF 81920
#define K1_BST 20480
#define K1_DUMMY (81920 + 3 * 20480)
#define K1_TLD 144
__device__ __forceinline__ float red8(float v) {
  v += dppf<0xB1, 0xf>(v);
  v += dppf<0x4E, 0xf>(v);
  v += dppf<0x141, 0xf>(v);
  return v;
}
__device__ __forceinline__ void k1_ln_rows(const u32x4 (&a)[10], char* atile, int row, int c8) {
  f32x2 v[40];
  f32x2 s2 = {0.f, 0.f};
#pragma unroll
  for (int jj = 0; jj < 10; ++jj)
#pragma unroll
    for (int c = 0; c < 4; ++c) {
      v[jj * 4 + c] = (f32x2){lo2f(a[jj][c]), hi2f(a[jj][c])};
      s2 += v[jj * 4 + c];
    }
  const float s = red8(s2[0] + s2[1]);
  const float mean = s * (1.0f / DM);
  const float mb = rbf(mean);
  const f32x2 mb2 = {mb, mb};
  f32x2 q2 = {0.f, 0.f};
#pragma unroll
  for (int i = 0; i < 40; ++i) {
    const f32x2 d = v[i] - mb2;
    q2 = __builtin_elementwise_fma(d, d, q2);
    const unsigned u = pk2(d[0], d[1]);
    v[i] = (f32x2){lo2f(u), hi2f(u)};
  }
  const float ss = red8(q2[0] + q2[1]);
  const float dm = mean - mb;
  const float var = fmaxf(ss * (1.0f / DM) - dm * dm, 0.f);
  const float vb = rbf(var);
  const float sd = rbf(sqrtf(rbf(vb + 1e-5f)));
  const float rs = 1.0f / sd;
  const f32x2 rs2 = {rs, rs};
  char* ap = atile + row * 128 + ((c8 ^ ((row >> 1) & 7)) << 4);
#pragma unroll
  for (int jj = 0; jj < 10; ++jj) {
    u32x4 o;
#pragma unroll
    for (int c = 0; c < 4; ++c) {
      const f32x2 n = v[jj * 4 + c] * rs2;
      o[c] = pk2(n[0], n[1]);
    }
    *(u32x4*)(ap + jj * 8192) = o;
  }
}
__global__ __launch_bounds__(512) void k1_ln_gemm(const u16* __restrict__ x, const u16* __restrict__ wre,
                                                   const u16* __restrict__ wim, u16* __restrict__ abT,
                                                   const u16* __restrict__ wsre, const u16* __restrict__ wsim) {
  __shared__ __attribute__((aligned(1024))) char smem[K1_DUMMY + 1024];
  const int tid = threadIdx.x, lane = tid & 63, q = lane >> 4, r = lane & 15;
  const int w = __builtin_amdgcn_readfirstlane(tid >> 6);
  const int xcd = blockIdx.x & 7, j = blockIdx.x >> 3;
  const int mt = (xcd >> 1) * 8 + (j & 7);
  const int nt = (xcd & 1) * 4 + (j >> 3);
  const int s0 = mt * 64;

  const int row8 = lane >> 3, c8 = lane & 7;
  const u16* W = (nt < 4) ? (wre + (size_t)nt * 160 * DM) : (wim + (size_t)(nt - 4) * 160 * DM);
  const int lw = w - 4;
  const char* pfp = (const char*)((xcd >> 2) ? wsim : wsre) + (size_t)(xcd & 3) * 512 * SEQ * 2 + (size_t)j * 65536 + lane * 16;
  const u16* xr = x + (size_t)(s0 + w * 8 + row8) * DM + c8 * 8;
#define K1_XLOAD()                                                                                  \
  _Pragma("unroll") for (int jj = 0; jj < 10; ++jj)                                                 \
    asm volatile("global_load_dwordx4 %0, %1, off offset:%2" : "=v"(a[jj]) : "v"(xr), "n"(jj * 128) : "memory")
#define K1_XWAIT(n)                                                                                 \
  asm volatile("s_waitcnt vmcnt(" #n ")"                                                            \
               : "+v"(a[0]), "+v"(a[1]), "+v"(a[2]), "+v"(a[3]), "+v"(a[4]), "+v"(a[5]), "+v"(a[6]), "+v"(a[7]), "+v"(a[8]), "+v"(a[9]) \
               :: "memory")
  if (w >= 4) {
    u32x4 a[10];
    K1_XLOAD();
    int boff[5];
#pragma unroll
    for (int i = 0; i < 5; ++i) {
      const int row = (lw * 5 + i) * 8 + row8;
      boff[i] = row * DM + ((c8 ^ ((row >> 1) & 7)) << 3);
    }
#define K1_ISSUE(stage, buf)                                                                        \
  do {                                                                                              \
    _Pragma("unroll") for (int i = 0; i < 5; ++i)                                                   \
      glds16_asm(W + boff[i] + (stage) * 64, lds_addr(smem) + K1_BOFF + (buf) * K1_BST + (lw * 5 + i) * 1024); \
  } while (0)
    K1_ISSUE(0, 0);
    K1_ISSUE(1, 1);
    K1_ISSUE(2, 2);
    K1_XWAIT(15);
    k1_ln_rows(a, smem + K1_AOFF, w * 8 + row8, c8);
    WAIT_V(10);
    WAIT_L(0);
    __builtin_amdgcn_s_barrier();
#pragma unroll
    for (int st = 0; st < 10; ++st) {
      if (st < 8) WAIT_V(5); else WAIT_V(0);
      __builtin_amdgcn_s_barrier();
      if (st + 3 < 10) K1_ISSUE(st + 3, st % 3);
    }
  } else {
    u32x4 a[10];
    K1_XLOAD();
    K1_XWAIT(0);
    k1_ln_rows(a, smem + K1_AOFF, w * 8 + row8, c8);
#pragma unroll
    for (int i = 0; i < 16; ++i) glds16_asm(pfp + (w * 16 + i) * 1024, lds_addr(smem) + K1_DUMMY);
    const int wm = w >> 1, wn = w & 1;
    f32x4 acc[2][5];
#pragma unroll
    for (int m = 0; m < 2; ++m)
#pragma unroll
      for (int n = 0; n < 5; ++n) acc[m][n] = (f32x4){0.f, 0.f, 0.f, 0.f};
    const int sw = (r >> 1) & 7;
    const int aro = K1_AOFF + (wm * 32 + r) * 128;
    const int bro = K1_BOFF + (wn * 80 + r) * 128;
    const int co0 = ((0 * 4 + q) ^ sw) << 4, co1 = ((1 * 4 + q) ^ sw) << 4;
    bf16x8 xa[2], xb[5], ya[2], yb[5];
#define K1_READ(fa, fb, st, h)                                                                      \
  do {                                                                                              \
    const int co_ = (h) ? co1 : co0;                                                                \
    _Pragma("unroll") for (int n = 0; n < 5; ++n)                                                   \
      fb[n] = *(const bf16x8*)(smem + bro + ((st) % 3) * K1_BST + n * 2048 + co_);                  \
    _Pragma("unroll") for (int m = 0; m < 2; ++m)                                                   \
      fa[m] = *(const bf16x8*)(smem + aro + (st) * 8192 + m * 2048 + co_);                          \
  } while (0)
#define K1_MMA(fa, fb)                                                                              \
  do {                                                                                              \
    _Pragma("unroll") for (int m = 0; m < 2; ++m)                                                   \
      _Pragma("unroll") for (int n = 0; n < 5; ++n)                                                 \
        acc[m][n] = __builtin_amdgcn_mfma_f32_16x16x32_bf16(fa[m], fb[n], acc[m][n], 0, 0, 0);      \
  } while (0)
#define SB() __builtin_amdgcn_sched_barrier(0)
    WAIT_L(0);
    __builtin_amdgcn_s_barrier();
    K1_READ(xa, xb, 0, 0);
#pragma unroll
    for (int st = 0; st < 10; ++st) {
      SB(); K1_READ(ya, yb, st, 1); SB(); K1_MMA(xa, xb);
      SB(); WAIT_L(0); __builtin_amdgcn_s_barrier(); SB();
      if (st < 9) K1_READ(xa, xb, st + 1, 0);
      SB(); K1_MMA(ya, yb);
    }
#pragma unroll
    for (int m = 0; m < 2; ++m)
#pragma unroll
      for (int n = 0; n < 5; ++n) {
        u32x2 o;
        o[0] = pk2(acc[m][n][0], acc[m][n][1]);
        o[1] = pk2(acc[m][n][2], acc[m][n][3]);
        *(u32x2*)(smem + (wn * 80 + n * 16 + r) * K1_TLD + (wm * 32 + m * 16 + q * 4) * 2) = o;
      }
  }
  WAIT_L(0);
  __builtin_amdgcn_s_barrier();
#pragma unroll
  for (int i = 0; i < 3; ++i) {
    const int c = tid + i * 512;
    if (c < 1280) {
      const int e = c >> 3, p = c & 7;
      const u32x4 o = *(const u32x4*)(smem + e * K1_TLD + p * 16);
      *(u32x4*)(abT + (size_t)(nt * 160 + e) * SEQ + s0 + p * 8) = o;
    }
  }
  WAIT_V(0);
}

#define K2_STAGE 53248
#define K2_TLD 176
__global__ __launch_bounds__(512) void k2_gemm(const u16* __restrict__ wsre, const u16* __restrict__ wsim,
                                                const u16* __restrict__ abT, u16* __restrict__ ypart) {
  __shared__ __attribute__((aligned(1024))) char smem[3 * K2_STAGE];
  const int tid = threadIdx.x, lane = tid & 63, q = lane >> 4, r = lane & 15;
  const int w = __builtin_amdgcn_readfirstlane(tid >> 6);
  const int xcd = blockIdx.x & 7, j = blockIdx.x >> 3;
  const int part = xcd >> 2;
  const int mt = (xcd & 3) * 4 + (j & 3);
  const int nt = j >> 2;
  const int t0 = mt * 128, e0 = nt * 80;

  if (w >= 4) {
    const int lw = w - 4;
    const u16* A = (part ? wsim : wsre) + (size_t)t0 * SEQ;
    const u16* B = abT + (size_t)(part * DM + e0) * SEQ;
    const int rin = lane >> 3, pos = lane & 7;
    int aoff[4], boff[5], bl[5];
#pragma unroll
    for (int p = 0; p < 4; ++p) {
      const int row = lw * 32 + p * 8 + rin;
      aoff[p] = row * SEQ + ((pos ^ ((row >> 1) & 7)) << 3);
    }
#pragma unroll
    for (int i = 0; i < 5; ++i) {
      const int pc = lw * 5 + i;
      const int sub = pc / 10, p = pc % 10;
      const int row = p * 8 + rin;
      boff[i] = row * SEQ + sub * 64 + ((pos ^ ((row >> 1) & 7)) << 3);
      bl[i] = 32768 + sub * 10240 + p * 1024;
    }
#define K2_ISSUE(stage, buf)                                                           \
  do {                                                                                 \
    char* sb_ = smem + (buf) * K2_STAGE;                                               \
    const int kb_ = (stage) * 128;                                                     \
    _Pragma("unroll") for (int sub = 0; sub < 2; ++sub)                                \
      _Pragma("unroll") for (int p = 0; p < 4; ++p)                                    \
        GLDS16(A + aoff[p] + kb_ + sub * 64, sb_ + sub * 16384 + (lw * 4 + p) * 1024); \
    _Pragma("unroll") for (int i = 0; i < 5; ++i)                                      \
      GLDS16(B + boff[i] + kb_, sb_ + bl[i]);                                          \
  } while (0)
    K2_ISSUE(0, 0);
    K2_ISSUE(1, 1);
    K2_ISSUE(2, 2);
    WAIT_V(26);
    __builtin_amdgcn_s_barrier();
    int buf = 0;
    for (int st = 0; st < 16; ++st) {
      if (st < 14) WAIT_V(13); else WAIT_V(0);
      __builtin_amdgcn_s_barrier();
      if (st + 3 < 16) K2_ISSUE(st + 3, buf);
      buf = buf + 1; if (buf >= 3) buf = 0;
    }
  } else {
    f32x4 acc[2][5];
#pragma unroll
    for (int m = 0; m < 2; ++m)
#pragma unroll
      for (int n = 0; n < 5; ++n) acc[m][n] = (f32x4){0.f, 0.f, 0.f, 0.f};
    const int sw = (r >> 1) & 7;
    const int aro = (w * 32 + r) * 128;
    const int bro = 32768 + r * 128;
    const int co0 = ((0 * 4 + q) ^ sw) << 4, co1 = ((1 * 4 + q) ^ sw) << 4;
    bf16x8 xa[2], xb[5], ya[2], yb[5];
#define K2_READ(fa, fb, sbp, s)                                                                              \
  do {                                                                                                       \
    const int co_ = ((s) & 1) ? co1 : co0;                                                                   \
    _Pragma("unroll") for (int n = 0; n < 5; ++n)                                                            \
      fb[n] = *(const bf16x8*)((sbp) + bro + ((s) >> 1) * 10240 + n * 2048 + co_);                           \
    _Pragma("unroll") for (int m = 0; m < 2; ++m)                                                            \
      fa[m] = *(const bf16x8*)((sbp) + aro + ((s) >> 1) * 16384 + m * 2048 + co_);                           \
  } while (0)
#define K2_MMA(fa, fb)                                                                                       \
  do {                                                                                                       \
    _Pragma("unroll") for (int m = 0; m < 2; ++m)                                                            \
      _Pragma("unroll") for (int n = 0; n < 5; ++n)                                                          \
        acc[m][n] = __builtin_amdgcn_mfma_f32_16x16x32_bf16(fb[n], fa[m], acc[m][n], 0, 0, 0);               \
  } while (0)
    __builtin_amdgcn_s_barrier();
    K2_READ(xa, xb, smem, 0);
    int buf = 0;
    for (int st = 0; st < 16; ++st) {
      const char* sb = smem + buf * K2_STAGE;
      int nb = buf + 1; if (nb >= 3) nb = 0;
      const char* sn = smem + nb * K2_STAGE;
      SB(); K2_READ(ya, yb, sb, 1); SB(); K2_MMA(xa, xb);
      SB(); K2_READ(xa, xb, sb, 2); SB(); K2_MMA(ya, yb);
      SB(); K2_READ(ya, yb, sb, 3); SB(); K2_MMA(xa, xb);
      SB(); WAIT_L(0); __builtin_amdgcn_s_barrier(); SB();
      if (st < 15) K2_READ(xa, xb, sn, 0);
      SB(); K2_MMA(ya, yb);
      buf = nb;
    }
#pragma unroll
    for (int m = 0; m < 2; ++m)
#pragma unroll
      for (int n = 0; n < 5; ++n) {
        u32x2 o;
        o[0] = pk2(acc[m][n][0], acc[m][n][1]);
        o[1] = pk2(acc[m][n][2], acc[m][n][3]);
        *(u32x2*)(smem + (w * 32 + m * 16 + r) * K2_TLD + (n * 16 + q * 4) * 2) = o;
      }
  }
  WAIT_L(0);
  __builtin_amdgcn_s_barrier();
  {
    u16* Y = ypart + (size_t)part * SEQ * DM;
#pragma unroll
    for (int i = 0; i < 3; ++i) {
      const int c = tid + i * 512;
      if (c < 1280) {
        const int t = c / 10, p = c % 10;
        const u32x4 o = *(const u32x4*)(smem + t * K2_TLD + p * 16);
        *(u32x4*)(Y + (size_t)(t0 + t) * DM + e0 + p * 8) = o;
      }
    }
  }
}

__global__ __launch_bounds__(576) void k3_ln2(const u16* __restrict__ x, const u16* __restrict__ ypart,
                                               const u16* __restrict__ wr, u16* __restrict__ out,
                                               float* __restrict__ rsc, float2* __restrict__ stats,
                                               const u16* __restrict__ wg, const u16* __restrict__ wu,
                                               const u16* __restrict__ wdn) {
  const int lane = threadIdx.x & 63;
  if (__builtin_amdgcn_readfirstlane(threadIdx.x >> 6) == 8) {
    __shared__ __attribute__((aligned(1024))) char dl[1024];
    const size_t fo = (size_t)blockIdx.x * 7680 + lane * 16;
#pragma unroll
    for (int i = 0; i < 7; ++i) {
      GLDS16((const char*)wg + fo + i * 1024, dl);
      GLDS16((const char*)wu + fo + i * 1024, dl);
    }
    if (lane < 32) {
      GLDS16((const char*)wg + fo + 7168, dl);
      GLDS16((const char*)wu + fo + 7168, dl);
    }
    WAIT_V(0);
    return;
  }
  const int row = blockIdx.x * 8 + (threadIdx.x >> 6);
  const unsigned* xp = (const unsigned*)(x + (size_t)row * DM);
  const unsigned* yr = (const unsigned*)(ypart + (size_t)row * DM);
  const unsigned* yi = (const unsigned*)(ypart + (size_t)SEQ * DM + (size_t)row * DM);
  const unsigned* wp = (const unsigned*)wr;
  unsigned xv[5], rv[5], iv[5], wv[5];
#pragma unroll
  for (int i = 0; i < 5; ++i) {
    xv[i] = xp[i * 64 + lane];
    rv[i] = yr[i * 64 + lane];
    iv[i] = yi[i * 64 + lane];
    wv[i] = wp[i * 64 + lane];
  }
  float h[10];
  float s = 0.f;
#pragma unroll
  for (int i = 0; i < 5; ++i) {
    const float y0 = rbf(lo2f(rv[i]) - lo2f(iv[i])), y1 = rbf(hi2f(rv[i]) - hi2f(iv[i]));
    h[2 * i] = rbf(lo2f(xv[i]) + y0);
    h[2 * i + 1] = rbf(hi2f(xv[i]) + y1);
    s += h[2 * i] + h[2 * i + 1];
  }
  s = wave_sum_f32(s);
  const float mean = s * (1.0f / DM);
  const float mb = rbf(mean);
  float d[10];
  float ss = 0.f;
#pragma unroll
  for (int i = 0; i < 10; ++i) {
    d[i] = h[i] - mb;
    ss += d[i] * d[i];
  }
  ss = wave_sum_f32(ss);
  const float dm = mean - mb;
  float var = fmaxf(ss * (1.0f / DM) - dm * dm, 0.f);
  const float vb = rbf(var);
  const float sd = rbf(sqrtf(rbf(vb + 1e-5f)));
  const float rs = 1.0f / sd;
  float dot = 0.f;
  unsigned* op = (unsigned*)(out + (size_t)row * DM);
#pragma unroll
  for (int i = 0; i < 5; ++i) {
    const float n0 = rbf(rbf(d[2 * i]) * rs), n1 = rbf(rbf(d[2 * i + 1]) * rs);
    dot += n0 * lo2f(wv[i]);
    dot += n1 * hi2f(wv[i]);
    op[i * 64 + lane] = pk2(h[2 * i] + n0, h[2 * i + 1] + n1);
  }
  dot = wave_sum_f32(dot);
  if (lane == 0) {
    rsc[row] = rbf(dot);
    stats[row] = make_float2(mb, rs);
  }
}

__device__ __forceinline__ bf16x8 ldtr_frag(const char* tile, int k0, int lane) {
  const int g = lane >> 4, i = lane & 15;
  const char* p = tile + (k0 + 8 * g + (i >> 2)) * 32 + (i & 3) * 8;
  s16x4 lo = __builtin_amdgcn_ds_read_tr16_b64_v4i16((__attribute__((address_space(3))) s16x4*)(p));
  s16x4 hi = __builtin_amdgcn_ds_read_tr16_b64_v4i16((__attribute__((address_space(3))) s16x4*)(p + 128));
  bf16x8 f;
  f[0] = lo[0]; f[1] = lo[1]; f[2] = lo[2]; f[3] = lo[3];
  f[4] = hi[0]; f[5] = hi[1]; f[6] = hi[2]; f[7] = hi[3];
  return f;
}

#define XS_LD 1296
#define K4_XS 40960
#define K4_RED (40960 + 8 * XS_LD)
#define K4_CAND (K4_RED + 4096)
__global__ __launch_bounds__(576) void k4_gateup(const u16* __restrict__ x, const u16* __restrict__ ypart,
                                                  const float* __restrict__ rsc, const float2* __restrict__ stats,
                                                  const u16* __restrict__ wg, const u16* __restrict__ wu,
                                                  unsigned* __restrict__ act, unsigned* __restrict__ hsel,
                                                  int* __restrict__ selg, float* __restrict__ gsel,
                                                  const u16* __restrict__ wd, char* __restrict__ wdt) {
  __shared__ __attribute__((aligned(1024))) char smem[K4_CAND + 256];
  const int tid = threadIdx.x, lane = tid & 63, q = lane >> 4, r = lane & 15;
  const int w = __builtin_amdgcn_readfirstlane(tid >> 6);
  if (blockIdx.x >= 96) {
    if (w == 8) return;
    const int b2 = blockIdx.x - 96;
    const char* srcp = (const char*)wd + (size_t)b2 * 16 * 1280;
    u32x4 v[3];
#pragma unroll
    for (int i = 0; i < 3; ++i) { const int c = i * 512 + tid; if (c < 1280) v[i] = __builtin_nontemporal_load((const u32x4*)(srcp + c * 16)); }
#pragma unroll
    for (int i = 0; i < 3; ++i) { const int c = i * 512 + tid; if (c < 1280) *(u32x4*)(smem + (c / 80) * 1296 + (c % 80) * 16) = v[i]; }
    WAIT_L(0);
    __builtin_amdgcn_s_barrier();
#pragma unroll
    for (int i = 0; i < 3; ++i) {
      const int d = i * 512 + tid;
      if (d < 1280) {
        const int cg = d >> 5, rw = (d & 31) >> 1, hf = d & 1;
        const u32x4 o = *(const u32x4*)(smem + rw * 1296 + cg * 32 + hf * 16);
        *(u32x4*)(wdt + (size_t)cg * 49152 + (b2 * 16 + rw) * 32 + hf * 16) = o;
      }
    }
    return;
  }
  const int xcd = blockIdx.x & 7, jj = blockIdx.x >> 3;
  const int f0 = ((jj >> 2) * 32 + xcd * 4 + (jj & 3)) * 16;
  if (w == 8) {
#pragma unroll 4
    for (int p = 0; p < 20; ++p) {
      const int row = p * 32 + (lane >> 1);
      GLDS16(wg + (size_t)row * FF + f0 + (lane & 1) * 8, smem + p * 1024);
      GLDS16(wu + (size_t)row * FF + f0 + (lane & 1) * 8, smem + 20480 + p * 1024);
    }
    __builtin_amdgcn_s_barrier();
    WAIT_V(0);
    return;
  }
  unsigned key[4];
  {
    const u32x4 v = *(const u32x4*)(rsc + tid * 4);
#pragma unroll
    for (int i = 0; i < 4; ++i) {
      const unsigned u = v[i];
      const unsigned o = (u & 0x80000000u) ? ~u : (u | 0x80000000u);
      key[i] = (o & 0xffff0000u) | (0xffffu - (unsigned)(tid * 4 + i));
    }
  }
  unsigned* cand = (unsigned*)(smem + K4_CAND);
#pragma unroll
  for (int rnd = 0; rnd < 8; ++rnd) {
    const unsigned lm = max(max(key[0], key[1]), max(key[2], key[3]));
    const unsigned wm = wave_max_u32(lm);
#pragma unroll
    for (int i = 0; i < 4; ++i) key[i] = (key[i] == wm) ? 0u : key[i];
    if (lane == 0) cand[w * 8 + rnd] = wm;
  }
  WAIT_L(0);
  __builtin_amdgcn_s_barrier();
  unsigned myk = 0;
  {
    unsigned k2 = cand[lane];
#pragma unroll
    for (int rnd = 0; rnd < 8; ++rnd) {
      const unsigned wm = wave_max_u32(k2);
      k2 = (k2 == wm) ? 0u : k2;
      myk = (w == rnd) ? wm : myk;
    }
  }
  {
    const int row = (int)(0xffffu - (myk & 0xffffu));
    const float2 st = stats[row];
    const unsigned* xp = (const unsigned*)(x + (size_t)row * DM);
    const unsigned* yr = (const unsigned*)(ypart + (size_t)row * DM);
    const unsigned* yi = (const unsigned*)(ypart + (size_t)SEQ * DM + (size_t)row * DM);
    unsigned xv[5], rv[5], iv[5];
#pragma unroll
    for (int i = 0; i < 5; ++i) { xv[i] = xp[i * 64 + lane]; rv[i] = yr[i * 64 + lane]; iv[i] = yi[i * 64 + lane]; }
#pragma unroll
    for (int i = 0; i < 5; ++i) {
      const float h0 = rbf(lo2f(xv[i]) + rbf(lo2f(rv[i]) - lo2f(iv[i])));
      const float h1 = rbf(hi2f(xv[i]) + rbf(hi2f(rv[i]) - hi2f(iv[i])));
      const unsigned hn = pk2(rbf(h0 - st.x) * st.y, rbf(h1 - st.x) * st.y);
      *(unsigned*)(smem + K4_XS + w * XS_LD + (i * 64 + lane) * 4) = hn;
      if (blockIdx.x == 0) {
        hsel[w * 320 + i * 64 + lane] = pk2(h0, h1);
        hsel[2560 + w * 320 + i * 64 + lane] = hn;
      }
    }
    if (blockIdx.x == 0 && lane == 0) {
      const unsigned ob = myk & 0xffff0000u;
      const float rr = __uint_as_float((ob & 0x80000000u) ? (ob & 0x7fffffffu) : (~ob & 0xffff0000u));
      selg[w] = row;
      gsel[w] = rbf(1.0f / (1.0f + __expf(-rr)));
    }
  }
  WAIT_L(0);
  __builtin_amdgcn_s_barrier();
  float* red = (float*)(smem + K4_RED);
  {
    const char* tile = smem + (w >> 2) * 20480;
    f32x4 acc = {0.f, 0.f, 0.f, 0.f};
#pragma unroll
    for (int ks = 0; ks < 5; ++ks) {
      const int k0 = (w & 3) * 160 + ks * 32;
      bf16x8 af = *(const bf16x8*)(smem + K4_XS + (r & 7) * XS_LD + (k0 + q * 8) * 2);
      if (r >= 8) af = (bf16x8){0, 0, 0, 0, 0, 0, 0, 0};
      const bf16x8 bw = ldtr_frag(tile, k0, lane);
      acc = __builtin_amdgcn_mfma_f32_16x16x32_bf16(af, bw, acc, 0, 0, 0);
    }
    if (lane < 32) *(f32x4*)(red + w * 128 + lane * 4) = acc;
  }
  WAIT_L(0);
  __builtin_amdgcn_s_barrier();
  if (tid < 64) {
    const int tk = tid >> 3, pr = tid & 7;
    float av[2];
#pragma unroll
    for (int e = 0; e < 2; ++e) {
      const int li = ((tk >> 2) * 16 + pr * 2 + e) * 4 + (tk & 3);
      float g = 0.f, u = 0.f;
#pragma unroll
      for (int ww = 0; ww < 4; ++ww) {
        g += red[ww * 128 + li];
        u += red[(ww + 4) * 128 + li];
      }
      g = rbf(g);
      u = rbf(u);
      const float sg = rbf(g / (1.0f + __expf(-g)));
      av[e] = sg * u;
    }
    act[tk * 768 + (f0 >> 1) + pr] = pk2(av[0], av[1]);
  }
}

#define AC_LD 3088
#define K5_AS 49152
#define K5_RED (49152 + 8 * AC_LD)
__global__ __launch_bounds__(576) void k5_down(const char* __restrict__ wdt, const u32x4* __restrict__ act,
                                                const unsigned* __restrict__ hsel, const int* __restrict__ selg,
                                                const float* __restrict__ gsel, u16* __restrict__ out) {
  __shared__ __attribute__((aligned(1024))) char smem[K5_RED + 4096];
  const int tid = threadIdx.x, lane = tid & 63, q = lane >> 4, r = lane & 15;
  const int w = __builtin_amdgcn_readfirstlane(tid >> 6);
  const int xcd = blockIdx.x & 7, jj = blockIdx.x >> 3;
  const int c0 = (xcd * 5 + jj) * 16;
  if (w == 8) {
#pragma unroll 4
    for (int p = 0; p < 48; ++p) GLDS16(wdt + (size_t)(c0 >> 4) * 49152 + p * 1024 + lane * 16, smem + p * 1024);
    WAIT_V(0);
    return;
  }
  u32x4 av[3];
#pragma unroll
  for (int i = 0; i < 3; ++i) av[i] = act[i * 512 + tid];
  const int tk = (tid >> 3) & 7, pr = tid & 7;
  const unsigned hv = hsel[tk * 320 + (c0 >> 1) + pr];
  const unsigned nv = hsel[2560 + tk * 320 + (c0 >> 1) + pr];
  const int orow = selg[tk];
  const float sgm = gsel[tk];
#pragma unroll
  for (int i = 0; i < 3; ++i) {
    const int idx = i * 512 + tid;
    *(u32x4*)(smem + K5_AS + (idx / 192) * AC_LD + (idx % 192) * 16) = av[i];
  }
  WAIT_L(0);
  __builtin_amdgcn_s_barrier();
  float* red = (float*)(smem + K5_RED);
  {
    f32x4 acc = {0.f, 0.f, 0.f, 0.f};
#pragma unroll
    for (int ks = 0; ks < 6; ++ks) {
      const int k0 = w * 192 + ks * 32;
      bf16x8 af = *(const bf16x8*)(smem + K5_AS + (r & 7) * AC_LD + (k0 + q * 8) * 2);
      if (r >= 8) af = (bf16x8){0, 0, 0, 0, 0, 0, 0, 0};
      const bf16x8 bw = ldtr_frag(smem, k0, lane);
      acc = __builtin_amdgcn_mfma_f32_16x16x32_bf16(af, bw, acc, 0, 0, 0);
    }
    if (lane < 32) *(f32x4*)(red + w * 128 + lane * 4) = acc;
  }
  WAIT_L(0);
  __builtin_amdgcn_s_barrier();
  if (tid < 64) {
    float ov[2];
#pragma unroll
    for (int e = 0; e < 2; ++e) {
      const int li = ((tk >> 2) * 16 + pr * 2 + e) * 4 + (tk & 3);
      float y = 0.f;
#pragma unroll
      for (int ww = 0; ww < 8; ++ww) y += red[ww * 128 + li];
      y = rbf(rbf(y) * sgm);
      const float h = e ? hi2f(hv) : lo2f(hv), hn = e ? hi2f(nv) : lo2f(nv);
      ov[e] = h + rbf(hn + y);
    }
    *(unsigned*)(out + (size_t)orow * DM + c0 + pr * 2) = pk2(ov[0], ov[1]);
  }
}

extern "C" void kernel_launch(void* const* d_in, const int* in_sizes, int n_in, void* d_out, int out_size,
                              void* d_ws, size_t ws_size, hipStream_t stream) {
  const u16* x = (const u16*)d_in[0];
  const u16* wsre = (const u16*)d_in[1];
  const u16* wsim = (const u16*)d_in[2];
  const u16* wdre = (const u16*)d_in[3];
  const u16* wdim = (const u16*)d_in[4];
  const u16* wr = (const u16*)d_in[6];
  const u16* wg = (const u16*)d_in[7];
  const u16* wu = (const u16*)d_in[8];
  const u16* wdn = (const u16*)d_in[9];
  u16* out = (u16*)d_out;
  char* ws = (char*)d_ws;
  u16* abT = (u16*)ws;
  u16* ypart = (u16*)(ws + 5242880);
  float* rsc = (float*)(ws + 10485760);
  float2* stats = (float2*)(ws + 10485760 + 8192);
  unsigned* act = (unsigned*)(ws + 10485760 + 8192 + 16384);
  unsigned* hsel = (unsigned*)(ws + 10485760 + 8192 + 16384 + 24576);
  int* selg = (int*)(ws + 10485760 + 8192 + 16384 + 24576 + 20480);
  float* gsel = (float*)(ws + 10485760 + 8192 + 16384 + 24576 + 20480 + 64);

  k1_ln_gemm<<<256, 512, 0, stream>>>(x, wdre, wdim, abT, wsre, wsim);
  k2_gemm<<<256, 512, 0, stream>>>(wsre, wsim, abT, ypart);
  k3_ln2<<<256, 576, 0, stream>>>(x, ypart, wr, out, rsc, stats, wg, wu, wdn);
  char* wdt = ws + (12u << 20);
  k4_gateup<<<192, 576, 0, stream>>>(x, ypart, rsc, stats, wg, wu, act, hsel, selg, gsel, wdn, wdt);
  k5_down<<<40, 576, 0, stream>>>(wdt, (const u32x4*)act, hsel, selg, gsel, out);
}
```

```cpp
#include <hip/hip_runtime.h>
#include <hip/hip_bf16.h>
#include <stdint.h>

#define PF0 6
#define PFS 1
#define SEQ 2048
#define DM 640
#define FF 1536

typedef unsigned short u16;
typedef __attribute__((ext_vector_type(8))) short bf16x8;
typedef __attribute__((ext_vector_type(4))) short s16x4;
typedef __attribute__((ext_vector_type(4))) float f32x4;
typedef __attribute__((ext_vector_type(2))) float f32x2;
typedef __attribute__((ext_vector_type(2))) __bf16 bf16x2_t;
typedef __attribute__((ext_vector_type(4))) unsigned u32x4;
typedef __attribute__((ext_vector_type(2))) unsigned u32x2;

__device__ __forceinline__ unsigned pk2(float a, float b) {
  f32x2 v = {a, b};
  bf16x2_t r = __builtin_convertvector(v, bf16x2_t);
  return __builtin_bit_cast(unsigned, r);
}
__device__ __forceinline__ float lo2f(unsigned w) { return __uint_as_float(w << 16); }
__device__ __forceinline__ float hi2f(unsigned w) { return __uint_as_float(w & 0xffff0000u); }
__device__ __forceinline__ float rbf(float a) { return lo2f(pk2(a, 0.f)); }
__device__ __forceinline__ float bf2f(u16 u) { return __uint_as_float(((unsigned)u) << 16); }

#define WAIT_V(n) asm volatile("s_waitcnt vmcnt(%0)" ::"n"(n) : "memory")
#define WAIT_L(n) asm volatile("s_waitcnt lgkmcnt(%0)" ::"n"(n) : "memory")

#define GLDS16(gptr, lptr) __builtin_amdgcn_global_load_lds((const unsigned*)(gptr), (unsigned*)(lptr), 16, 0, 0)

__device__ __forceinline__ void glds16_asm(const void* gsrc, unsigned lds_dst) {
  unsigned keep;
  asm volatile("s_mov_b32 %0, m0\n\ts_mov_b32 m0, %2\n\ts_nop 0\n\tglobal_load_lds_dwordx4 %1, off\n\ts_mov_b32 m0, %0"
               : "=&s"(keep) : "v"(gsrc), "s"(lds_dst) : "memory");
}
__device__ __forceinline__ unsigned lds_addr(const char* p) {
  return (unsigned)(uintptr_t)(const __attribute__((address_space(3))) char*)p;
}

__device__ __forceinline__ unsigned wave_max_u32(unsigned v) {
  v = max(v, (unsigned)__builtin_amdgcn_update_dpp(0, (int)v, 0x111, 0xf, 0xf, false));
  v = max(v, (unsigned)__builtin_amdgcn_update_dpp(0, (int)v, 0x112, 0xf, 0xf, false));
  v = max(v, (unsigned)__builtin_amdgcn_update_dpp(0, (int)v, 0x114, 0xf, 0xf, false));
  v = max(v, (unsigned)__builtin_amdgcn_update_dpp(0, (int)v, 0x118, 0xf, 0xf, false));
  v = max(v, (unsigned)__builtin_amdgcn_update_dpp(0, (int)v, 0x142, 0xa, 0xf, false));
  v = max(v, (unsigned)__builtin_amdgcn_update_dpp(0, (int)v, 0x143, 0xc, 0xf, false));
  return (unsigned)__builtin_amdgcn_readlane((int)v, 63);
}
template <int CTRL, int RM>
__device__ __forceinline__ float dppf(float v) {
  return __int_as_float(__builtin_amdgcn_update_dpp(0, __float_as_int(v), CTRL, RM, 0xf, false));
}
__device__ __forceinline__ float wave_sum_f32(float v) {
  v += dppf<0x111, 0xf>(v);
  v += dppf<0x112, 0xf>(v);
  v += dppf<0x114, 0xf>(v);
  v += dppf<0x118, 0xf>(v);
  v += dppf<0x142, 0xa>(v);
  v += dppf<0x143, 0xc>(v);
  return __int_as_float(__builtin_amdgcn_readlane(__float_as_int(v), 63));
}

#define K1_AOFF 0
#define K1_BOFF 81920
#define K1_BST 20480
#define K1_LDS (81920 + 4 * 20480)
#define K1_TLD 144
__device__ __forceinline__ float red8(float v) {
  v += dppf<0xB1, 0xf>(v);
  v += dppf<0x4E, 0xf>(v);
  v += dppf<0x141, 0xf>(v);
  return v;
}
__device__ __forceinline__ void k1_ln_rows(const u32x4 (&a)[10], char* atile, int row, int c8) {
  f32x2 v[40];
  f32x2 s2 = {0.f, 0.f};
#pragma unroll
  for (int jj = 0; jj < 10; ++jj)
#pragma unroll
    for (int c = 0; c < 4; ++c) {
      v[jj * 4 + c] = (f32x2){lo2f(a[jj][c]), hi2f(a[jj][c])};
      s2 += v[jj * 4 + c];
    }
  const float s = red8(s2[0] + s2[1]);
  const float mean = s * (1.0f / DM);
  const float mb = rbf(mean);
  const f32x2 mb2 = {mb, mb};
  f32x2 q2 = {0.f, 0.f};
#pragma unroll
  for (int i = 0; i < 40; ++i) {
    const f32x2 d = v[i] - mb2;
    q2 = __builtin_elementwise_fma(d, d, q2);
    const unsigned u = pk2(d[0], d[1]);
    v[i] = (f32x2){lo2f(u), hi2f(u)};
  }
  const float ss = red8(q2[0] + q2[1]);
  const float dm = mean - mb;
  const float var = fmaxf(ss * (1.0f / DM) - dm * dm, 0.f);
  const float vb = rbf(var);
  const float sd = rbf(sqrtf(rbf(vb + 1e-5f)));
  const float rs = 1.0f / sd;
  const f32x2 rs2 = {rs, rs};
  char* ap = atile + row * 128 + ((c8 ^ ((row >> 1) & 7)) << 4);
#pragma unroll
  for (int jj = 0; jj < 10; ++jj) {
    u32x4 o;
#pragma unroll
    for (int c = 0; c < 4; ++c) {
      const f32x2 n = v[jj * 4 + c] * rs2;
      o[c] = pk2(n[0], n[1]);
    }
    *(u32x4*)(ap + jj * 8192) = o;
  }
}
__global__ __launch_bounds__(512) void k1_ln_gemm(const u16* __restrict__ x, const u16* __restrict__ wre,
                                                   const u16* __restrict__ wim, u16* __restrict__ abT,
                                                   const u16* __restrict__ wsre, const u16* __restrict__ wsim,
                                                   const u16* __restrict__ wr) {
  __shared__ __attribute__((aligned(1024))) char smem[K1_LDS];
  const int tid = threadIdx.x, lane = tid & 63, q = lane >> 4, r = lane & 15;
  const int w = __builtin_amdgcn_readfirstlane(tid >> 6);
  const int xcd = blockIdx.x & 7, j = blockIdx.x >> 3;
  const int mt = (xcd >> 1) * 8 + (j & 7);
  const int nt = (xcd & 1) * 4 + (j >> 3);
  const int s0 = mt * 64;

  const int row8 = lane >> 3, c8 = lane & 7;
  const u16* W = (nt < 4) ? (wre + (size_t)nt * 160 * DM) : (wim + (size_t)(nt - 4) * 160 * DM);
  const int lw = w - 4;
  const char* pfp = (const char*)((xcd >> 2) ? wsim : wsre) + (size_t)(xcd & 3) * 512 * SEQ * 2 + (size_t)j * 65536 + lane * 16;
  const u16* xr = x + (size_t)(s0 + w * 8 + row8) * DM + c8 * 8;
  u32x4 pfd = {0u, 0u, 0u, 0u};
#define K1_XLOAD()                                                                                  \
  _Pragma("unroll") for (int jj = 0; jj < 10; ++jj)                                                 \
    asm volatile("global_load_dwordx4 %0, %1, off offset:%2" : "=v"(a[jj]) : "v"(xr), "n"(jj * 128) : "memory")
#define K1_XWAIT(n)                                                                                 \
  asm volatile("s_waitcnt vmcnt(" #n ")"                                                            \
               : "+v"(a[0]), "+v"(a[1]), "+v"(a[2]), "+v"(a[3]), "+v"(a[4]), "+v"(a[5]), "+v"(a[6]), "+v"(a[7]), "+v"(a[8]), "+v"(a[9]) \
               :: "memory")
  if (w >= 4) {
    u32x4 a[10];
    K1_XLOAD();
    int boff[5];
#pragma unroll
    for (int i = 0; i < 5; ++i) {
      const int row = (lw * 5 + i) * 8 + row8;
      boff[i] = row * DM + ((c8 ^ ((row >> 1) & 7)) << 3);
    }
#define K1_ISSUE(stage, buf)                                                                        \
  do {                                                                                              \
    _Pragma("unroll") for (int i = 0; i < 5; ++i)                                                   \
      glds16_asm(W + boff[i] + (stage) * 64, lds_addr(smem) + K1_BOFF + (buf) * K1_BST + (lw * 5 + i) * 1024); \
  } while (0)
    K1_ISSUE(0, 0);
    K1_ISSUE(1, 1);
    K1_ISSUE(2, 2);
    K1_ISSUE(3, 3);
    K1_XWAIT(20);
    k1_ln_rows(a, smem + K1_AOFF, w * 8 + row8, c8);
    WAIT_V(15);
    WAIT_L(0);
    __builtin_amdgcn_s_barrier();
#pragma unroll
    for (int st = 0; st < 10; ++st) {
      if (st < 7) WAIT_V(10); else if (st == 7) WAIT_V(5); else WAIT_V(0);
      __builtin_amdgcn_s_barrier();
      if (st + 4 < 10) K1_ISSUE(st + 4, st % 4);
    }
  } else {
    u32x4 a[10];
    K1_XLOAD();
    K1_XWAIT(0);
    k1_ln_rows(a, smem + K1_AOFF, w * 8 + row8, c8);
#define K1_PF(i) asm volatile("global_load_dwordx4 %0, %1, off" : "+v"(pfd) : "v"(pfp + (w * 16 + (i)) * 1024) : "memory")
    if (lane < 80) asm volatile("global_load_dwordx4 %0, %1, off" : "+v"(pfd) : "v"((const char*)wr + (lane < 80 ? lane : 0) * 16) : "memory");
#pragma unroll
    for (int i = 0; i < PF0; ++i) K1_PF(i);
    const int wm = w >> 1, wn = w & 1;
    f32x4 acc[2][5];
#pragma unroll
    for (int m = 0; m < 2; ++m)
#pragma unroll
      for (int n = 0; n < 5; ++n) acc[m][n] = (f32x4){0.f, 0.f, 0.f, 0.f};
    const int sw = (r >> 1) & 7;
    const int aro = K1_AOFF + (wm * 32 + r) * 128;
    const int bro = K1_BOFF + (wn * 80 + r) * 128;
    const int co0 = ((0 * 4 + q) ^ sw) << 4, co1 = ((1 * 4 + q) ^ sw) << 4;
    bf16x8 xa[2], xb[5], ya[2], yb[5];
#define K1_READ(fa, fb, st, h)                                                                      \
  do {                                                                                              \
    const int co_ = (h) ? co1 : co0;                                                                \
    _Pragma("unroll") for (int n = 0; n < 5; ++n)                                                   \
      fb[n] = *(const bf16x8*)(smem + bro + ((st) % 4) * K1_BST + n * 2048 + co_);                  \
    _Pragma("unroll") for (int m = 0; m < 2; ++m)                                                   \
      fa[m] = *(const bf16x8*)(smem + aro + (st) * 8192 + m * 2048 + co_);                          \
  } while (0)
#define K1_MMA(fa, fb)                                                                              \
  do {                                                                                              \
    _Pragma("unroll") for (int m = 0; m < 2; ++m)                                                   \
      _Pragma("unroll") for (int n = 0; n < 5; ++n)                                                 \
        acc[m][n] = __builtin_amdgcn_mfma_f32_16x16x32_bf16(fa[m], fb[n], acc[m][n], 0, 0, 0);      \
  } while (0)
#define SB() __builtin_amdgcn_sched_barrier(0)
    WAIT_L(0);
    __builtin_amdgcn_s_barrier();
    K1_READ(xa, xb, 0, 0);
#pragma unroll
    for (int st = 0; st < 10; ++st) {
      SB(); K1_READ(ya, yb, st, 1); SB(); K1_MMA(xa, xb);
      SB(); WAIT_L(0); __builtin_amdgcn_s_barrier(); SB();
      if (st < 9) K1_READ(xa, xb, st + 1, 0);
      _Pragma("unroll") for (int i = PF0 + st * PFS; i < PF0 + (st + 1) * PFS; ++i) if (i < 16) K1_PF(i);
      SB(); K1_MMA(ya, yb);
    }
#pragma unroll
    for (int m = 0; m < 2; ++m)
#pragma unroll
      for (int n = 0; n < 5; ++n) {
        u32x2 o;
        o[0] = pk2(acc[m][n][0], acc[m][n][1]);
        o[1] = pk2(acc[m][n][2], acc[m][n][3]);
        *(u32x2*)(smem + (wn * 80 + n * 16 + r) * K1_TLD + (wm * 32 + m * 16 + q * 4) * 2) = o;
      }
  }
  WAIT_L(0);
  __builtin_amdgcn_s_barrier();
#pragma unroll
  for (int i = 0; i < 3; ++i) {
    const int c = tid + i * 512;
    if (c < 1280) {
      const int e = c >> 3, p = c & 7;
      const u32x4 o = *(const u32x4*)(smem + e * K1_TLD + p * 16);
      *(u32x4*)(abT + (size_t)(nt * 160 + e) * SEQ + s0 + p * 8) = o;
    }
  }
  asm volatile("s_waitcnt vmcnt(0)" : "+v"(pfd) :: "memory");
}

#define K2_STAGE 26624
#define K2_NBUF 6
#define K2_TLD 176
__global__ __launch_bounds__(512) void k2_gemm(const u16* __restrict__ wsre, const u16* __restrict__ wsim,
                                                const u16* __restrict__ abT, u16* __restrict__ ypart) {
  __shared__ __attribute__((aligned(1024))) char smem[K2_NBUF * K2_STAGE];
  const int tid = threadIdx.x, lane = tid & 63, q = lane >> 4, r = lane & 15;
  const int w = __builtin_amdgcn_readfirstlane(tid >> 6);
  const int xcd = blockIdx.x & 7, j = blockIdx.x >> 3;
  const int part = xcd >> 2;
  const int mt = (xcd & 3) * 4 + (j & 3);
  const int nt = j >> 2;
  const int t0 = mt * 128, e0 = nt * 80;

  if (w >= 4) {
    const int lw = w - 4;
    const u16* A = (part ? wsim : wsre) + (size_t)t0 * SEQ;
    const u16* B = abT + (size_t)(part * DM + e0) * SEQ;
    const int rin = lane >> 3, pos = lane & 7;
    int aoff[4], boff[3];
#pragma unroll
    for (int p = 0; p < 4; ++p) {
      const int row = lw * 32 + p * 8 + rin;
      aoff[p] = row * SEQ + ((pos ^ ((row >> 1) & 7)) << 3);
    }
    const bool three = lw < 2;
    const int pb = three ? lw * 3 : 6 + (lw - 2) * 2;
#pragma unroll
    for (int i = 0; i < 3; ++i) {
      const int row = (pb + i) * 8 + rin;
      boff[i] = row * SEQ + ((pos ^ ((row >> 1) & 7)) << 3);
    }
#define K2_ISSUE(stage, buf)                                                           \
  do {                                                                                 \
    char* sb_ = smem + (buf) * K2_STAGE;                                               \
    const int kb_ = (stage) * 64;                                                      \
    _Pragma("unroll") for (int p = 0; p < 4; ++p)                                      \
      GLDS16(A + aoff[p] + kb_, sb_ + (lw * 4 + p) * 1024);                            \
    GLDS16(B + boff[0] + kb_, sb_ + 16384 + pb * 1024);                                \
    GLDS16(B + boff[1] + kb_, sb_ + 16384 + (pb + 1) * 1024);                          \
    if (three) GLDS16(B + boff[2] + kb_, sb_ + 16384 + (pb + 2) * 1024);               \
  } while (0)
#pragma unroll
    for (int s = 0; s < K2_NBUF; ++s) K2_ISSUE(s, s);
    if (three) WAIT_V(35); else WAIT_V(30);
    __builtin_amdgcn_s_barrier();
    int buf = 0;
    for (int st = 0; st < 26; ++st) {
      if (three) WAIT_V(28); else WAIT_V(24);
      __builtin_amdgcn_s_barrier();
      K2_ISSUE(st + K2_NBUF, buf);
      buf = buf + 1; if (buf >= K2_NBUF) buf = 0;
    }
    if (three) WAIT_V(28); else WAIT_V(24);
    __builtin_amdgcn_s_barrier();
    if (three) WAIT_V(21); else WAIT_V(18);
    __builtin_amdgcn_s_barrier();
    if (three) WAIT_V(14); else WAIT_V(12);
    __builtin_amdgcn_s_barrier();
    if (three) WAIT_V(7); else WAIT_V(6);
    __builtin_amdgcn_s_barrier();
    WAIT_V(0);
    __builtin_amdgcn_s_barrier();
    __builtin_amdgcn_s_barrier();
  } else {
    f32x4 acc[2][5];
#pragma unroll
    for (int m = 0; m < 2; ++m)
#pragma unroll
      for (int n = 0; n < 5; ++n) acc[m][n] = (f32x4){0.f, 0.f, 0.f, 0.f};
    const int sw = (r >> 1) & 7;
    const int aro = (w * 32 + r) * 128;
    const int bro = 16384 + r * 128;
    const int co0 = ((0 * 4 + q) ^ sw) << 4, co1 = ((1 * 4 + q) ^ sw) << 4;
    bf16x8 xa[2], xb[5], ya[2], yb[5];
#define K2_READ(fa, fb, sbp, h)                                                                              \
  do {                                                                                                       \
    const int co_ = (h) ? co1 : co0;                                                                         \
    _Pragma("unroll") for (int n = 0; n < 5; ++n)                                                            \
      fb[n] = *(const bf16x8*)((sbp) + bro + n * 2048 + co_);                                                \
    _Pragma("unroll") for (int m = 0; m < 2; ++m)                                                            \
      fa[m] = *(const bf16x8*)((sbp) + aro + m * 2048 + co_);                                                \
  } while (0)
#define K2_MMA(fa, fb)                                                                                       \
  do {                                                                                                       \
    _Pragma("unroll") for (int m = 0; m < 2; ++m)                                                            \
      _Pragma("unroll") for (int n = 0; n < 5; ++n)                                                          \
        acc[m][n] = __builtin_amdgcn_mfma_f32_16x16x32_bf16(fb[n], fa[m], acc[m][n], 0, 0, 0);               \
  } while (0)
    __builtin_amdgcn_s_barrier();
    K2_READ(xa, xb, smem, 0);
    for (int so = 0; so < 30; so += K2_NBUF) {
#pragma unroll
      for (int b = 0; b < K2_NBUF; ++b) {
        const char* sb = smem + b * K2_STAGE;
        const char* sn = smem + ((b + 1) % K2_NBUF) * K2_STAGE;
        SB(); K2_READ(ya, yb, sb, 1); SB(); K2_MMA(xa, xb);
        SB(); WAIT_L(0); __builtin_amdgcn_s_barrier(); SB();
        K2_READ(xa, xb, sn, 0);
        SB(); K2_MMA(ya, yb);
      }
    }
    SB(); K2_READ(ya, yb, smem, 1); SB(); K2_MMA(xa, xb);
    SB(); WAIT_L(0); __builtin_amdgcn_s_barrier(); SB();
    K2_READ(xa, xb, smem + K2_STAGE, 0);
    SB(); K2_MMA(ya, yb);
    SB(); K2_READ(ya, yb, smem + K2_STAGE, 1); SB(); K2_MMA(xa, xb);
    SB(); WAIT_L(0); __builtin_amdgcn_s_barrier(); SB();
    K2_MMA(ya, yb);
#pragma unroll
    for (int m = 0; m < 2; ++m)
#pragma unroll
      for (int n = 0; n < 5; ++n) {
        u32x2 o;
        o[0] = pk2(acc[m][n][0], acc[m][n][1]);
        o[1] = pk2(acc[m][n][2], acc[m][n][3]);
        *(u32x2*)(smem + (w * 32 + m * 16 + r) * K2_TLD + (n * 16 + q * 4) * 2) = o;
      }
  }
  WAIT_L(0);
  __builtin_amdgcn_s_barrier();
  {
    u16* Y = ypart + (size_t)part * SEQ * DM;
#pragma unroll
    for (int i = 0; i < 3; ++i) {
      const int c = tid + i * 512;
      if (c < 1280) {
        const int t = c / 10, p = c % 10;
        const u32x4 o = *(const u32x4*)(smem + t * K2_TLD + p * 16);
        *(u32x4*)(Y + (size_t)(t0 + t) * DM + e0 + p * 8) = o;
      }
    }
  }
}

__global__ __launch_bounds__(576) void k3_ln2(const u16* __restrict__ x, const u16* __restrict__ ypart,
                                               const u16* __restrict__ wr, u16* __restrict__ out,
                                               float* __restrict__ rsc, float2* __restrict__ stats,
                                               const u16* __restrict__ wg, const u16* __restrict__ wu,
                                               const u16* __restrict__ wdn) {
  const int lane = threadIdx.x & 63;
  if (__builtin_amdgcn_readfirstlane(threadIdx.x >> 6) == 8) {
    __shared__ __attribute__((aligned(1024))) char dl[1024];
    const size_t fo = (size_t)blockIdx.x * 7680 + lane * 16;
#pragma unroll
    for (int i = 0; i < 7; ++i) {
      GLDS16((const char*)wg + fo + i * 1024, dl);
      GLDS16((const char*)wu + fo + i * 1024, dl);
    }
    if (lane < 32) {
      GLDS16((const char*)wg + fo + 7168, dl);
      GLDS16((const char*)wu + fo + 7168, dl);
    }
    WAIT_V(0);
    return;
  }
  const int row = blockIdx.x * 8 + (threadIdx.x >> 6);
  const unsigned* xp = (const unsigned*)(x + (size_t)row * DM);
  const unsigned* yr = (const unsigned*)(ypart + (size_t)row * DM);
  const unsigned* yi = (const unsigned*)(ypart + (size_t)SEQ * DM + (size_t)row * DM);
  const unsigned* wp = (const unsigned*)wr;
  unsigned xv[5], rv[5], iv[5], wv[5];
#pragma unroll
  for (int i = 0; i < 5; ++i) {
    xv[i] = xp[i * 64 + lane];
    rv[i] = yr[i * 64 + lane];
    iv[i] = yi[i * 64 + lane];
    wv[i] = wp[i * 64 + lane];
  }
  float h[10];
  float s = 0.f;
#pragma unroll
  for (int i = 0; i < 5; ++i) {
    const float y0 = rbf(lo2f(rv[i]) - lo2f(iv[i])), y1 = rbf(hi2f(rv[i]) - hi2f(iv[i]));
    h[2 * i] = rbf(lo2f(xv[i]) + y0);
    h[2 * i + 1] = rbf(hi2f(xv[i]) + y1);
    s += h[2 * i] + h[2 * i + 1];
  }
  s = wave_sum_f32(s);
  const float mean = s * (1.0f / DM);
  const float mb = rbf(mean);
  float d[10];
  float ss = 0.f;
#pragma unroll
  for (int i = 0; i < 10; ++i) {
    d[i] = h[i] - mb;
    ss += d[i] * d[i];
  }
  ss = wave_sum_f32(ss);
  const float dm = mean - mb;
  float var = fmaxf(ss * (1.0f / DM) - dm * dm, 0.f);
  const float vb = rbf(var);
  const float sd = rbf(sqrtf(rbf(vb + 1e-5f)));
  const float rs = 1.0f / sd;
  float dot = 0.f;
  unsigned* op = (unsigned*)(out + (size_t)row * DM);
#pragma unroll
  for (int i = 0; i < 5; ++i) {
    const float n0 = rbf(rbf(d[2 * i]) * rs), n1 = rbf(rbf(d[2 * i + 1]) * rs);
    dot += n0 * lo2f(wv[i]);
    dot += n1 * hi2f(wv[i]);
    op[i * 64 + lane] = pk2(h[2 * i] + n0, h[2 * i + 1] + n1);
  }
  dot = wave_sum_f32(dot);
  if (lane == 0) {
    rsc[row] = rbf(dot);
    stats[row] = make_float2(mb, rs);
  }
}

__device__ __forceinline__ bf16x8 ldtr_frag(const char* tile, int k0, int lane) {
  const int g = lane >> 4, i = lane & 15;
  const char* p = tile + (k0 + 8 * g + (i >> 2)) * 32 + (i & 3) * 8;
  s16x4 lo = __builtin_amdgcn_ds_read_tr16_b64_v4i16((__attribute__((address_space(3))) s16x4*)(p));
  s16x4 hi = __builtin_amdgcn_ds_read_tr16_b64_v4i16((__attribute__((address_space(3))) s16x4*)(p + 128));
  bf16x8 f;
  f[0] = lo[0]; f[1] = lo[1]; f[2] = lo[2]; f[3] = lo[3];
  f[4] = hi[0]; f[5] = hi[1]; f[6] = hi[2]; f[7] = hi[3];
  return f;
}

#define XS_LD 1296
#define K4_XS 40960
#define K4_RED (40960 + 8 * XS_LD)
#define K4_CAND (K4_RED + 4096)
__global__ __launch_bounds__(576) void k4_gateup(const u16* __restrict__ x, const u16* __restrict__ ypart,
                                                  const float* __restrict__ rsc, const float2* __restrict__ stats,
                                                  const u16* __restrict__ wg, const u16* __restrict__ wu,
                                                  unsigned* __restrict__ act, unsigned* __restrict__ hsel,
                                                  int* __restrict__ selg, float* __restrict__ gsel,
                                                  const u16* __restrict__ wd, char* __restrict__ wdt) {
  __shared__ __attribute__((aligned(1024))) char smem[K4_CAND + 256];
  const int tid = threadIdx.x, lane = tid & 63, q = lane >> 4, r = lane & 15;
  const int w = __builtin_amdgcn_readfirstlane(tid >> 6);
  if (blockIdx.x >= 96) {
    if (w == 8) return;
    const int b2 = blockIdx.x - 96;
    const char* srcp = (const char*)wd + (size_t)b2 * 16 * 1280;
    u32x4 v[3];
#pragma unroll
    for (int i = 0; i < 3; ++i) { const int c = i * 512 + tid; if (c < 1280) v[i] = __builtin_nontemporal_load((const u32x4*)(srcp + c * 16)); }
#pragma unroll
    for (int i = 0; i < 3; ++i) { const int c = i * 512 + tid; if (c < 1280) *(u32x4*)(smem + (c / 80) * 1296 + (c % 80) * 16) = v[i]; }
    WAIT_L(0);
    __builtin_amdgcn_s_barrier();
#pragma unroll
    for (int i = 0; i < 3; ++i) {
      const int d = i * 512 + tid;
      if (d < 1280) {
        const int cg = d >> 5, rw = (d & 31) >> 1, hf = d & 1;
        const u32x4 o = *(const u32x4*)(smem + rw * 1296 + cg * 32 + hf * 16);
        *(u32x4*)(wdt + (size_t)cg * 49152 + (b2 * 16 + rw) * 32 + hf * 16) = o;
      }
    }
    return;
  }
  const int xcd = blockIdx.x & 7, jj = blockIdx.x >> 3;
  const int f0 = ((jj >> 2) * 32 + xcd * 4 + (jj & 3)) * 16;
  if (w == 8) {
#pragma unroll 4
    for (int p = 0; p < 20; ++p) {
      const int row = p * 32 + (lane >> 1);
      GLDS16(wg + (size_t)row * FF + f0 + (lane & 1) * 8, smem + p * 1024);
      GLDS16(wu + (size_t)row * FF + f0 + (lane & 1) * 8, smem + 20480 + p * 1024);
    }
    __builtin_amdgcn_s_barrier();
    WAIT_V(0);
    return;
  }
  unsigned key[4];
  {
    const u32x4 v = *(const u32x4*)(rsc + tid * 4);
#pragma unroll
    for (int i = 0; i < 4; ++i) {
      const unsigned u = v[i];
      const unsigned o = (u & 0x80000000u) ? ~u : (u | 0x80000000u);
      key[i] = (o & 0xffff0000u) | (0xffffu - (unsigned)(tid * 4 + i));
    }
  }
  unsigned* cand = (unsigned*)(smem + K4_CAND);
#pragma unroll
  for (int rnd = 0; rnd < 8; ++rnd) {
    const unsigned lm = max(max(key[0], key[1]), max(key[2], key[3]));
    const unsigned wm = wave_max_u32(lm);
#pragma unroll
    for (int i = 0; i < 4; ++i) key[i] = (key[i] == wm) ? 0u : key[i];
    if (lane == 0) cand[w * 8 + rnd] = wm;
  }
  WAIT_L(0);
  __builtin_amdgcn_s_barrier();
  unsigned myk = 0;
  {
    unsigned k2 = cand[lane];
#pragma unroll
    for (int rnd = 0; rnd < 8; ++rnd) {
      const unsigned wm = wave_max_u32(k2);
      k2 = (k2 == wm) ? 0u : k2;
      myk = (w == rnd) ? wm : myk;
    }
  }
  {
    const int row = (int)(0xffffu - (myk & 0xffffu));
    const float2 st = stats[row];
    const unsigned* xp = (const unsigned*)(x + (size_t)row * DM);
    const unsigned* yr = (const unsigned*)(ypart + (size_t)row * DM);
    const unsigned* yi = (const unsigned*)(ypart + (size_t)SEQ * DM + (size_t)row * DM);
    unsigned xv[5], rv[5], iv[5];
#pragma unroll
    for (int i = 0; i < 5; ++i) { xv[i] = xp[i * 64 + lane]; rv[i] = yr[i * 64 + lane]; iv[i] = yi[i * 64 + lane]; }
#pragma unroll
    for (int i = 0; i < 5; ++i) {
      const float h0 = rbf(lo2f(xv[i]) + rbf(lo2f(rv[i]) - lo2f(iv[i])));
      const float h1 = rbf(hi2f(xv[i]) + rbf(hi2f(rv[i]) - hi2f(iv[i])));
      const unsigned hn = pk2(rbf(h0 - st.x) * st.y, rbf(h1 - st.x) * st.y);
      *(unsigned*)(smem + K4_XS + w * XS_LD + (i * 64 + lane) * 4) = hn;
      if (blockIdx.x == 0) {
        hsel[w * 320 + i * 64 + lane] = pk2(h0, h1);
        hsel[2560 + w * 320 + i * 64 + lane] = hn;
      }
    }
    if (blockIdx.x == 0 && lane == 0) {
      const unsigned ob = myk & 0xffff0000u;
      const float rr = __uint_as_float((ob & 0x80000000u) ? (ob & 0x7fffffffu) : (~ob & 0xffff0000u));
      selg[w] = row;
      gsel[w] = rbf(1.0f / (1.0f + __expf(-rr)));
    }
  }
  WAIT_L(0);
  __builtin_amdgcn_s_barrier();
  float* red = (float*)(smem + K4_RED);
  {
    const char* tile = smem + (w >> 2) * 20480;
    f32x4 acc = {0.f, 0.f, 0.f, 0.f};
#pragma unroll
    for (int ks = 0; ks < 5; ++ks) {
      const int k0 = (w & 3) * 160 + ks * 32;
      bf16x8 af = *(const bf16x8*)(smem + K4_XS + (r & 7) * XS_LD + (k0 + q * 8) * 2);
      if (r >= 8) af = (bf16x8){0, 0, 0, 0, 0, 0, 0, 0};
      const bf16x8 bw = ldtr_frag(tile, k0, lane);
      acc = __builtin_amdgcn_mfma_f32_16x16x32_bf16(af, bw, acc, 0, 0, 0);
    }
    if (lane < 32) *(f32x4*)(red + w * 128 + lane * 4) = acc;
  }
  WAIT_L(0);
  __builtin_amdgcn_s_barrier();
  if (tid < 64) {
    const int tk = tid >> 3, pr = tid & 7;
    float av[2];
#pragma unroll
    for (int e = 0; e < 2; ++e) {
      const int li = ((tk >> 2) * 16 + pr * 2 + e) * 4 + (tk & 3);
      float g = 0.f, u = 0.f;
#pragma unroll
      for (int ww = 0; ww < 4; ++ww) {
        g += red[ww * 128 + li];
        u += red[(ww + 4) * 128 + li];
      }
      g = rbf(g);
      u = rbf(u);
      const float sg = rbf(g / (1.0f + __expf(-g)));
      av[e] = sg * u;
    }
    act[tk * 768 + (f0 >> 1) + pr] = pk2(av[0], av[1]);
  }
}

#define AC_LD 3088
#define K5_AS 49152
#define K5_RED (49152 + 8 * AC_LD)
__global__ __launch_bounds__(576) void k5_down(const char* __restrict__ wdt, const u32x4* __restrict__ act,
                                                const unsigned* __restrict__ hsel, const int* __restrict__ selg,
                                                const float* __restrict__ gsel, u16* __restrict__ out) {
  __shared__ __attribute__((aligned(1024))) char smem[K5_RED + 4096];
  const int tid = threadIdx.x, lane = tid & 63, q = lane >> 4, r = lane & 15;
  const int w = __builtin_amdgcn_readfirstlane(tid >> 6);
  const int xcd = blockIdx.x & 7, jj = blockIdx.x >> 3;
  const int c0 = (xcd * 5 + jj) * 16;
  if (w == 8) {
#pragma unroll 4
    for (int p = 0; p < 48; ++p) GLDS16(wdt + (size_t)(c0 >> 4) * 49152 + p * 1024 + lane * 16, smem + p * 1024);
    WAIT_V(0);
    return;
  }
  u32x4 av[3];
#pragma unroll
  for (int i = 0; i < 3; ++i) av[i] = act[i * 512 + tid];
  const int tk = (tid >> 3) & 7, pr = tid & 7;
  const unsigned hv = hsel[tk * 320 + (c0 >> 1) + pr];
  const unsigned nv = hsel[2560 + tk * 320 + (c0 >> 1) + pr];
  const int orow = selg[tk];
  const float sgm = gsel[tk];
#pragma unroll
  for (int i = 0; i < 3; ++i) {
    const int idx = i * 512 + tid;
    *(u32x4*)(smem + K5_AS + (idx / 192) * AC_LD + (idx % 192) * 16) = av[i];
  }
  WAIT_L(0);
  __builtin_amdgcn_s_barrier();
  float* red = (float*)(smem + K5_RED);
  {
    f32x4 acc = {0.f, 0.f, 0.f, 0.f};
#pragma unroll
    for (int ks = 0; ks < 6; ++ks) {
      const int k0 = w * 192 + ks * 32;
      bf16x8 af = *(const bf16x8*)(smem + K5_AS + (r & 7) * AC_LD + (k0 + q * 8) * 2);
      if (r >= 8) af = (bf16x8){0, 0, 0, 0, 0, 0, 0, 0};
      const bf16x8 bw = ldtr_frag(smem, k0, lane);
      acc = __builtin_amdgcn_mfma_f32_16x16x32_bf16(af, bw, acc, 0, 0, 0);
    }
    if (lane < 32) *(f32x4*)(red + w * 128 + lane * 4) = acc;
  }
  WAIT_L(0);
  __builtin_amdgcn_s_barrier();
  if (tid < 64) {
    float ov[2];
#pragma unroll
    for (int e = 0; e < 2; ++e) {
      const int li = ((tk >> 2) * 16 + pr * 2 + e) * 4 + (tk & 3);
      float y = 0.f;
#pragma unroll
      for (int ww = 0; ww < 8; ++ww) y += red[ww * 128 + li];
      y = rbf(rbf(y) * sgm);
      const float h = e ? hi2f(hv) : lo2f(hv), hn = e ? hi2f(nv) : lo2f(nv);
      ov[e] = h + rbf(hn + y);
    }
    *(unsigned*)(out + (size_t)orow * DM + c0 + pr * 2) = pk2(ov[0], ov[1]);
  }
}

extern "C" void kernel_launch(void* const* d_in, const int* in_sizes, int n_in, void* d_out, int out_size,
                              void* d_ws, size_t ws_size, hipStream_t stream) {
  const u16* x = (const u16*)d_in[0];
  const u16* wsre = (const u16*)d_in[1];
  const u16* wsim = (const u16*)d_in[2];
  const u16* wdre = (const u16*)d_in[3];
  const u16* wdim = (const u16*)d_in[4];
  const u16* wr = (const u16*)d_in[6];
  const u16* wg = (const u16*)d_in[7];
  const u16* wu = (const u16*)d_in[8];
  const u16* wdn = (const u16*)d_in[9];
  u16* out = (u16*)d_out;
  char* ws = (char*)d_ws;
  u16* abT = (u16*)ws;
  u16* ypart = (u16*)(ws + 5242880);
  float* rsc = (float*)(ws + 10485760);
  float2* stats = (float2*)(ws + 10485760 + 8192);
  unsigned* act = (unsigned*)(ws + 10485760 + 8192 + 16384);
  unsigned* hsel = (unsigned*)(ws + 10485760 + 8192 + 16384 + 24576);
  int* selg = (int*)(ws + 10485760 + 8192 + 16384 + 24576 + 20480);
  float* gsel = (float*)(ws + 10485760 + 8192 + 16384 + 24576 + 20480 + 64);

  k1_ln_gemm<<<256, 512, 0, stream>>>(x, wdre, wdim, abT, wsre, wsim, wr);
  k2_gemm<<<256, 512, 0, stream>>>(wsre, wsim, abT, ypart);
  k3_ln2<<<256, 576, 0, stream>>>(x, ypart, wr, out, rsc, stats, wg, wu, wdn);
  char* wdt = ws + (12u << 20);
  k4_gateup<<<192, 576, 0, stream>>>(x, ypart, rsc, stats, wg, wu, act, hsel, selg, gsel, wdn, wdt);
  k5_down<<<40, 576, 0, stream>>>(wdt, (const u32x4*)act, hsel, selg, gsel, out);
}
```

```cpp
#include <hip/hip_runtime.h>
#include <hip/hip_bf16.h>
#include <stdint.h>

#define PF0 6
#define PFS 1
#define SEQ 2048
#define DM 640
#define FF 1536

typedef unsigned short u16;
typedef __attribute__((ext_vector_type(8))) short bf16x8;
typedef __attribute__((ext_vector_type(4))) short s16x4;
typedef __attribute__((ext_vector_type(4))) float f32x4;
typedef __attribute__((ext_vector_type(2))) float f32x2;
typedef __attribute__((ext_vector_type(2))) __bf16 bf16x2_t;
typedef __attribute__((ext_vector_type(4))) unsigned u32x4;
typedef __attribute__((ext_vector_type(2))) unsigned u32x2;

__device__ __forceinline__ unsigned pk2(float a, float b) {
  f32x2 v = {a, b};
  bf16x2_t r = __builtin_convertvector(v, bf16x2_t);
  return __builtin_bit_cast(unsigned, r);
}
__device__ __forceinline__ float lo2f(unsigned w) { return __uint_as_float(w << 16); }
__device__ __forceinline__ float hi2f(unsigned w) { return __uint_as_float(w & 0xffff0000u); }
__device__ __forceinline__ float rbf(float a) { return lo2f(pk2(a, 0.f)); }
__device__ __forceinline__ float bf2f(u16 u) { return __uint_as_float(((unsigned)u) << 16); }

#define WAIT_V(n) asm volatile("s_waitcnt vmcnt(%0)" ::"n"(n) : "memory")
#define WAIT_L(n) asm volatile("s_waitcnt lgkmcnt(%0)" ::"n"(n) : "memory")

#define GLDS16(gptr, lptr) __builtin_amdgcn_global_load_lds((const unsigned*)(gptr), (unsigned*)(lptr), 16, 0, 0)

__device__ __forceinline__ void glds16_asm(const void* gsrc, unsigned lds_dst) {
  unsigned keep;
  asm volatile("s_mov_b32 %0, m0\n\ts_mov_b32 m0, %2\n\ts_nop 0\n\tglobal_load_lds_dwordx4 %1, off\n\ts_mov_b32 m0, %0"
               : "=&s"(keep) : "v"(gsrc), "s"(lds_dst) : "memory");
}
__device__ __forceinline__ unsigned lds_addr(const char* p) {
  return (unsigned)(uintptr_t)(const __attribute__((address_space(3))) char*)p;
}

__device__ __forceinline__ unsigned wave_max_u32(unsigned v) {
  v = max(v, (unsigned)__builtin_amdgcn_update_dpp(0, (int)v, 0x111, 0xf, 0xf, false));
  v = max(v, (unsigned)__builtin_amdgcn_update_dpp(0, (int)v, 0x112, 0xf, 0xf, false));
  v = max(v, (unsigned)__builtin_amdgcn_update_dpp(0, (int)v, 0x114, 0xf, 0xf, false));
  v = max(v, (unsigned)__builtin_amdgcn_update_dpp(0, (int)v, 0x118, 0xf, 0xf, false));
  v = max(v, (unsigned)__builtin_amdgcn_update_dpp(0, (int)v, 0x142, 0xa, 0xf, false));
  v = max(v, (unsigned)__builtin_amdgcn_update_dpp(0, (int)v, 0x143, 0xc, 0xf, false));
  return (unsigned)__builtin_amdgcn_readlane((int)v, 63);
}
template <int CTRL, int RM>
__device__ __forceinline__ float dppf(float v) {
  return __int_as_float(__builtin_amdgcn_update_dpp(0, __float_as_int(v), CTRL, RM, 0xf, false));
}
__device__ __forceinline__ float wave_sum_f32(float v) {
  v += dppf<0x111, 0xf>(v);
  v += dppf<0x112, 0xf>(v);
  v += dppf<0x114, 0xf>(v);
  v += dppf<0x118, 0xf>(v);
  v += dppf<0x142, 0xa>(v);
  v += dppf<0x143, 0xc>(v);
  return __int_as_float(__builtin_amdgcn_readlane(__float_as_int(v), 63));
}

#define K1_AOFF 0
#define K1_BOFF 81920
#define K1_BST 20480
#define K1_LDS (81920 + 4 * 20480)
#define K1_TLD 144
__device__ __forceinline__ float red8(float v) {
  v += dppf<0xB1, 0xf>(v);
  v += dppf<0x4E, 0xf>(v);
  v += dppf<0x141, 0xf>(v);
  return v;
}
__device__ __forceinline__ void k1_ln_rows(const u32x4 (&a)[10], char* atile, int row, int c8) {
  f32x2 v[40];
  f32x2 s2 = {0.f, 0.f};
#pragma unroll
  for (int jj = 0; jj < 10; ++jj)
#pragma unroll
    for (int c = 0; c < 4; ++c) {
      v[jj * 4 + c] = (f32x2){lo2f(a[jj][c]), hi2f(a[jj][c])};
      s2 += v[jj * 4 + c];
    }
  const float s = red8(s2[0] + s2[1]);
  const float mean = s * (1.0f / DM);
  const float mb = rbf(mean);
  const f32x2 mb2 = {mb, mb};
  f32x2 q2 = {0.f, 0.f};
#pragma unroll
  for (int i = 0; i < 40; ++i) {
    const f32x2 d = v[i] - mb2;
    q2 = __builtin_elementwise_fma(d, d, q2);
    const unsigned u = pk2(d[0], d[1]);
    v[i] = (f32x2){lo2f(u), hi2f(u)};
  }
  const float ss = red8(q2[0] + q2[1]);
  const float dm = mean - mb;
  const float var = fmaxf(ss * (1.0f / DM) - dm * dm, 0.f);
  const float vb = rbf(var);
  const float sd = rbf(sqrtf(rbf(vb + 1e-5f)));
  const float rs = 1.0f / sd;
  const f32x2 rs2 = {rs, rs};
  char* ap = atile + row * 128 + ((c8 ^ ((row >> 1) & 7)) << 4);
#pragma unroll
  for (int jj = 0; jj < 10; ++jj) {
    u32x4 o;
#pragma unroll
    for (int c = 0; c < 4; ++c) {
      const f32x2 n = v[jj * 4 + c] * rs2;
      o[c] = pk2(n[0], n[1]);
    }
    *(u32x4*)(ap + jj * 8192) = o;
  }
}
__global__ __launch_bounds__(512) void k1_ln_gemm(const u16* __restrict__ x, const u16* __restrict__ wre,
                                                   const u16* __restrict__ wim, u16* __restrict__ abT,
                                                   const u16* __restrict__ wsre, const u16* __restrict__ wsim) {
  __shared__ __attribute__((aligned(1024))) char smem[K1_LDS];
  const int tid = threadIdx.x, lane = tid & 63, q = lane >> 4, r = lane & 15;
  const int w = __builtin_amdgcn_readfirstlane(tid >> 6);
  const int xcd = blockIdx.x & 7, j = blockIdx.x >> 3;
  const int mt = (xcd >> 1) * 8 + (j & 7);
  const int nt = (xcd & 1) * 4 + (j >> 3);
  const int s0 = mt * 64;

  const int row8 = lane >> 3, c8 = lane & 7;
  const u16* W = (nt < 4) ? (wre + (size_t)nt * 160 * DM) : (wim + (size_t)(nt - 4) * 160 * DM);
  const int lw = w - 4;
  const char* pfp = (const char*)((xcd >> 2) ? wsim : wsre) + (size_t)(xcd & 3) * 512 * SEQ * 2 + (size_t)j * 65536 + lane * 16;
  const u16* xr = x + (size_t)(s0 + w * 8 + row8) * DM + c8 * 8;
  u32x4 pfd = {0u, 0u, 0u, 0u};
#define K1_XLOAD()                                                                                  \
  _Pragma("unroll") for (int jj = 0; jj < 10; ++jj)                                                 \
    asm volatile("global_load_dwordx4 %0, %1, off offset:%2" : "=v"(a[jj]) : "v"(xr), "n"(jj * 128) : "memory")
#define K1_XWAIT(n)                                                                                 \
  asm volatile("s_waitcnt vmcnt(" #n ")"                                                            \
               : "+v"(a[0]), "+v"(a[1]), "+v"(a[2]), "+v"(a[3]), "+v"(a[4]), "+v"(a[5]), "+v"(a[6]), "+v"(a[7]), "+v"(a[8]), "+v"(a[9]) \
               :: "memory")
  if (w >= 4) {
    u32x4 a[10];
    K1_XLOAD();
    int boff[5];
#pragma unroll
    for (int i = 0; i < 5; ++i) {
      const int row = (lw * 5 + i) * 8 + row8;
      boff[i] = row * DM + ((c8 ^ ((row >> 1) & 7)) << 3);
    }
#define K1_ISSUE(stage, buf)                                                                        \
  do {                                                                                              \
    _Pragma("unroll") for (int i = 0; i < 5; ++i)                                                   \
      glds16_asm(W + boff[i] + (stage) * 64, lds_addr(smem) + K1_BOFF + (buf) * K1_BST + (lw * 5 + i) * 1024); \
  } while (0)
    K1_ISSUE(0, 0);
    K1_ISSUE(1, 1);
    K1_ISSUE(2, 2);
    K1_ISSUE(3, 3);
    K1_XWAIT(20);
    k1_ln_rows(a, smem + K1_AOFF, w * 8 + row8, c8);
    WAIT_V(15);
    WAIT_L(0);
    __builtin_amdgcn_s_barrier();
#pragma unroll
    for (int st = 0; st < 10; ++st) {
      if (st < 7) WAIT_V(10); else if (st == 7) WAIT_V(5); else WAIT_V(0);
      __builtin_amdgcn_s_barrier();
      if (st + 4 < 10) K1_ISSUE(st + 4, st % 4);
    }
  } else {
    u32x4 a[10];
    K1_XLOAD();
    K1_XWAIT(0);
    k1_ln_rows(a, smem + K1_AOFF, w * 8 + row8, c8);
#define K1_PF(i) asm volatile("global_load_dwordx4 %0, %1, off" : "+v"(pfd) : "v"(pfp + (w * 16 + (i)) * 1024) : "memory")
#pragma unroll
    for (int i = 0; i < PF0; ++i) K1_PF(i);
    const int wm = w >> 1, wn = w & 1;
    f32x4 acc[2][5];
#pragma unroll
    for (int m = 0; m < 2; ++m)
#pragma unroll
      for (int n = 0; n < 5; ++n) acc[m][n] = (f32x4){0.f, 0.f, 0.f, 0.f};
    const int sw = (r >> 1) & 7;
    const int aro = K1_AOFF + (wm * 32 + r) * 128;
    const int bro = K1_BOFF + (wn * 80 + r) * 128;
    const int co0 = ((0 * 4 + q) ^ sw) << 4, co1 = ((1 * 4 + q) ^ sw) << 4;
    bf16x8 xa[2], xb[5], ya[2], yb[5];
#define K1_READ(fa, fb, st, h)                                                                      \
  do {                                                                                              \
    const int co_ = (h) ? co1 : co0;                                                                \
    _Pragma("unroll") for (int n = 0; n < 5; ++n)                                                   \
      fb[n] = *(const bf16x8*)(smem + bro + ((st) % 4) * K1_BST + n * 2048 + co_);                  \
    _Pragma("unroll") for (int m = 0; m < 2; ++m)                                                   \
      fa[m] = *(const bf16x8*)(smem + aro + (st) * 8192 + m * 2048 + co_);                          \
  } while (0)
#define K1_MMA(fa, fb)                                                                              \
  do {                                                                                              \
    _Pragma("unroll") for (int m = 0; m < 2; ++m)                                                   \
      _Pragma("unroll") for (int n = 0; n < 5; ++n)                                                 \
        acc[m][n] = __builtin_amdgcn_mfma_f32_16x16x32_bf16(fa[m], fb[n], acc[m][n], 0, 0, 0);      \
  } while (0)
#define SB() __builtin_amdgcn_sched_barrier(0)
    WAIT_L(0);
    __builtin_amdgcn_s_barrier();
    K1_READ(xa, xb, 0, 0);
#pragma unroll
    for (int st = 0; st < 10; ++st) {
      SB(); K1_READ(ya, yb, st, 1); SB(); K1_MMA(xa, xb);
      SB(); WAIT_L(0); __builtin_amdgcn_s_barrier(); SB();
      if (st < 9) K1_READ(xa, xb, st + 1, 0);
      _Pragma("unroll") for (int i = PF0 + st * PFS; i < PF0 + (st + 1) * PFS; ++i) if (i < 16) K1_PF(i);
      SB(); K1_MMA(ya, yb);
    }
#pragma unroll
    for (int m = 0; m < 2; ++m)
#pragma unroll
      for (int n = 0; n < 5; ++n) {
        u32x2 o;
        o[0] = pk2(acc[m][n][0], acc[m][n][1]);
        o[1] = pk2(acc[m][n][2], acc[m][n][3]);
        *(u32x2*)(smem + (wn * 80 + n * 16 + r) * K1_TLD + (wm * 32 + m * 16 + q * 4) * 2) = o;
      }
  }
  WAIT_L(0);
  __builtin_amdgcn_s_barrier();
#pragma unroll
  for (int i = 0; i < 3; ++i) {
    const int c = tid + i * 512;
    if (c < 1280) {
      const int e = c >> 3, p = c & 7;
      const u32x4 o = *(const u32x4*)(smem + e * K1_TLD + p * 16);
      *(u32x4*)(abT + (size_t)(nt * 160 + e) * SEQ + s0 + p * 8) = o;
    }
  }
  asm volatile("s_waitcnt vmcnt(0)" : "+v"(pfd) :: "memory");
}

#define K2_STAGE 26624
#define K2_NBUF 6
#define K2_TLD 176
__global__ __launch_bounds__(512) void k2_gemm(const u16* __restrict__ wsre, const u16* __restrict__ wsim,
                                                const u16* __restrict__ abT, u16* __restrict__ ypart) {
  __shared__ __attribute__((aligned(1024))) char smem[K2_NBUF * K2_STAGE];
  const int tid = threadIdx.x, lane = tid & 63, q = lane >> 4, r = lane & 15;
  const int w = __builtin_amdgcn_readfirstlane(tid >> 6);
  const int xcd = blockIdx.x & 7, j = blockIdx.x >> 3;
  const int part = xcd >> 2;
  const int mt = (xcd & 3) * 4 + (j & 3);
  const int nt = j >> 2;
  const int t0 = mt * 128, e0 = nt * 80;

  if (w >= 4) {
    const int lw = w - 4;
    const u16* A = (part ? wsim : wsre) + (size_t)t0 * SEQ;
    const u16* B = abT + (size_t)(part * DM + e0) * SEQ;
    const int rin = lane >> 3, pos = lane & 7;
    int aoff[4], boff[3];
#pragma unroll
    for (int p = 0; p < 4; ++p) {
      const int row = lw * 32 + p * 8 + rin;
      aoff[p] = row * SEQ + ((pos ^ ((row >> 1) & 7)) << 3);
    }
    const bool three = lw < 2;
    const int pb = three ? lw * 3 : 6 + (lw - 2) * 2;
#pragma unroll
    for (int i = 0; i < 3; ++i) {
      const int row = (pb + i) * 8 + rin;
      boff[i] = row * SEQ + ((pos ^ ((row >> 1) & 7)) << 3);
    }
#define K2_ISSUE(stage, buf)                                                           \
  do {                                                                                 \
    char* sb_ = smem + (buf) * K2_STAGE;                                               \
    const int kb_ = (stage) * 64;                                                      \
    _Pragma("unroll") for (int p = 0; p < 4; ++p)                                      \
      GLDS16(A + aoff[p] + kb_, sb_ + (lw * 4 + p) * 1024);                            \
    GLDS16(B + boff[0] + kb_, sb_ + 16384 + pb * 1024);                                \
    GLDS16(B + boff[1] + kb_, sb_ + 16384 + (pb + 1) * 1024);                          \
    if (three) GLDS16(B + boff[2] + kb_, sb_ + 16384 + (pb + 2) * 1024);               \
  } while (0)
#pragma unroll
    for (int s = 0; s < K2_NBUF; ++s) K2_ISSUE(s, s);
    if (three) WAIT_V(35); else WAIT_V(30);
    __builtin_amdgcn_s_barrier();
    int buf = 0;
    for (int st = 0; st < 26; ++st) {
      if (three) WAIT_V(28); else WAIT_V(24);
      __builtin_amdgcn_s_barrier();
      K2_ISSUE(st + K2_NBUF, buf);
      buf = buf + 1; if (buf >= K2_NBUF) buf = 0;
    }
    if (three) WAIT_V(28); else WAIT_V(24);
    __builtin_amdgcn_s_barrier();
    if (three) WAIT_V(21); else WAIT_V(18);
    __builtin_amdgcn_s_barrier();
    if (three) WAIT_V(14); else WAIT_V(12);
    __builtin_amdgcn_s_barrier();
    if (three) WAIT_V(7); else WAIT_V(6);
    __builtin_amdgcn_s_barrier();
    WAIT_V(0);
    __builtin_amdgcn_s_barrier();
    __builtin_amdgcn_s_barrier();
  } else {
    f32x4 acc[2][5];
#pragma unroll
    for (int m = 0; m < 2; ++m)
#pragma unroll
      for (int n = 0; n < 5; ++n) acc[m][n] = (f32x4){0.f, 0.f, 0.f, 0.f};
    const int sw = (r >> 1) & 7;
    const int aro = (w * 32 + r) * 128;
    const int bro = 16384 + r * 128;
    const int co0 = ((0 * 4 + q) ^ sw) << 4, co1 = ((1 * 4 + q) ^ sw) << 4;
    bf16x8 xa[2], xb[5], ya[2], yb[5];
#define K2_READ(fa, fb, sbp, h)                                                                              \
  do {                                                                                                       \
    const int co_ = (h) ? co1 : co0;                                                                         \
    _Pragma("unroll") for (int n = 0; n < 5; ++n)                                                            \
      fb[n] = *(const bf16x8*)((sbp) + bro + n * 2048 + co_);                                                \
    _Pragma("unroll") for (int m = 0; m < 2; ++m)                                                            \
      fa[m] = *(const bf16x8*)((sbp) + aro + m * 2048 + co_);                                                \
  } while (0)
#define K2_MMA(fa, fb)                                                                                       \
  do {                                                                                                       \
    _Pragma("unroll") for (int m = 0; m < 2; ++m)                                                            \
      _Pragma("unroll") for (int n = 0; n < 5; ++n)                                                          \
        acc[m][n] = __builtin_amdgcn_mfma_f32_16x16x32_bf16(fb[n], fa[m], acc[m][n], 0, 0, 0);               \
  } while (0)
    __builtin_amdgcn_s_barrier();
    K2_READ(xa, xb, smem, 0);
    for (int so = 0; so < 30; so += K2_NBUF) {
#pragma unroll
      for (int b = 0; b < K2_NBUF; ++b) {
        const char* sb = smem + b * K2_STAGE;
        const char* sn = smem + ((b + 1) % K2_NBUF) * K2_STAGE;
        SB(); K2_READ(ya, yb, sb, 1); SB(); K2_MMA(xa, xb);
        SB(); WAIT_L(0); __builtin_amdgcn_s_barrier(); SB();
        K2_READ(xa, xb, sn, 0);
        SB(); K2_MMA(ya, yb);
      }
    }
    SB(); K2_READ(ya, yb, smem, 1); SB(); K2_MMA(xa, xb);
    SB(); WAIT_L(0); __builtin_amdgcn_s_barrier(); SB();
    K2_READ(xa, xb, smem + K2_STAGE, 0);
    SB(); K2_MMA(ya, yb);
    SB(); K2_READ(ya, yb, smem + K2_STAGE, 1); SB(); K2_MMA(xa, xb);
    SB(); WAIT_L(0); __builtin_amdgcn_s_barrier(); SB();
    K2_MMA(ya, yb);
#pragma unroll
    for (int m = 0; m < 2; ++m)
#pragma unroll
      for (int n = 0; n < 5; ++n) {
        u32x2 o;
        o[0] = pk2(acc[m][n][0], acc[m][n][1]);
        o[1] = pk2(acc[m][n][2], acc[m][n][3]);
        *(u32x2*)(smem + (w * 32 + m * 16 + r) * K2_TLD + (n * 16 + q * 4) * 2) = o;
      }
  }
  WAIT_L(0);
  __builtin_amdgcn_s_barrier();
  {
    u16* Y = ypart + (size_t)part * SEQ * DM;
#pragma unroll
    for (int i = 0; i < 3; ++i) {
      const int c = tid + i * 512;
      if (c < 1280) {
        const int t = c / 10, p = c % 10;
        const u32x4 o = *(const u32x4*)(smem + t * K2_TLD + p * 16);
        *(u32x4*)(Y + (size_t)(t0 + t) * DM + e0 + p * 8) = o;
      }
    }
  }
}

__global__ __launch_bounds__(576) void k3_ln2(const u16* __restrict__ x, const u16* __restrict__ ypart,
                                               const u16* __restrict__ wr, u16* __restrict__ out,
                                               float* __restrict__ rsc, float2* __restrict__ stats,
                                               const u16* __restrict__ wg, const u16* __restrict__ wu,
                                               const u16* __restrict__ wdn) {
  const int lane = threadIdx.x & 63;
  if (__builtin_amdgcn_readfirstlane(threadIdx.x >> 6) == 8) {
    __shared__ __attribute__((aligned(1024))) char dl[1024];
    const size_t fo = (size_t)blockIdx.x * 7680 + lane * 16;
#pragma unroll
    for (int i = 0; i < 7; ++i) {
      GLDS16((const char*)wg + fo + i * 1024, dl);
      GLDS16((const char*)wu + fo + i * 1024, dl);
    }
    if (lane < 32) {
      GLDS16((const char*)wg + fo + 7168, dl);
      GLDS16((const char*)wu + fo + 7168, dl);
    }
    WAIT_V(0);
    return;
  }
  const int row = blockIdx.x * 8 + (threadIdx.x >> 6);
  const unsigned* xp = (const unsigned*)(x + (size_t)row * DM);
  const unsigned* yr = (const unsigned*)(ypart + (size_t)row * DM);
  const unsigned* yi = (const unsigned*)(ypart + (size_t)SEQ * DM + (size_t)row * DM);
  const unsigned* wp = (const unsigned*)wr;
  unsigned xv[5], rv[5], iv[5], wv[5];
#pragma unroll
  for (int i = 0; i < 5; ++i) {
    xv[i] = xp[i * 64 + lane];
    rv[i] = yr[i * 64 + lane];
    iv[i] = yi[i * 64 + lane];
    wv[i] = wp[i * 64 + lane];
  }
  float h[10];
  float s = 0.f;
#pragma unroll
  for (int i = 0; i < 5; ++i) {
    const float y0 = rbf(lo2f(rv[i]) - lo2f(iv[i])), y1 = rbf(hi2f(rv[i]) - hi2f(iv[i]));
    h[2 * i] = rbf(lo2f(xv[i]) + y0);
    h[2 * i + 1] = rbf(hi2f(xv[i]) + y1);
    s += h[2 * i] + h[2 * i + 1];
  }
  s = wave_sum_f32(s);
  const float mean = s * (1.0f / DM);
  const float mb = rbf(mean);
  float d[10];
  float ss = 0.f;
#pragma unroll
  for (int i = 0; i < 10; ++i) {
    d[i] = h[i] - mb;
    ss += d[i] * d[i];
  }
  ss = wave_sum_f32(ss);
  const float dm = mean - mb;
  float var = fmaxf(ss * (1.0f / DM) - dm * dm, 0.f);
  const float vb = rbf(var);
  const float sd = rbf(sqrtf(rbf(vb + 1e-5f)));
  const float rs = 1.0f / sd;
  float dot = 0.f;
  unsigned* op = (unsigned*)(out + (size_t)row * DM);
#pragma unroll
  for (int i = 0; i < 5; ++i) {
    const float n0 = rbf(rbf(d[2 * i]) * rs), n1 = rbf(rbf(d[2 * i + 1]) * rs);
    dot += n0 * lo2f(wv[i]);
    dot += n1 * hi2f(wv[i]);
    op[i * 64 + lane] = pk2(h[2 * i] + n0, h[2 * i + 1] + n1);
  }
  dot = wave_sum_f32(dot);
  if (lane == 0) {
    rsc[row] = rbf(dot);
    stats[row] = make_float2(mb, rs);
  }
}

__device__ __forceinline__ bf16x8 ldtr_frag(const char* tile, int k0, int lane) {
  const int g = lane >> 4, i = lane & 15;
  const char* p = tile + (k0 + 8 * g + (i >> 2)) * 32 + (i & 3) * 8;
  s16x4 lo = __builtin_amdgcn_ds_read_tr16_b64_v4i16((__attribute__((address_space(3))) s16x4*)(p));
  s16x4 hi = __builtin_amdgcn_ds_read_tr16_b64_v4i16((__attribute__((address_space(3))) s16x4*)(p + 128));
  bf16x8 f;
  f[0] = lo[0]; f[1] = lo[1]; f[2] = lo[2]; f[3] = lo[3];
  f[4] = hi[0]; f[5] = hi[1]; f[6] = hi[2]; f[7] = hi[3];
  return f;
}

#define XS_LD 1296
#define K4_XS 40960
#define K4_RED (40960 + 8 * XS_LD)
#define K4_CAND (K4_RED + 4096)
__global__ __launch_bounds__(576) void k4_gateup(const u16* __restrict__ x, const u16* __restrict__ ypart,
                                                  const float* __restrict__ rsc, const float2* __restrict__ stats,
                                                  const u16* __restrict__ wg, const u16* __restrict__ wu,
                                                  unsigned* __restrict__ act, unsigned* __restrict__ hsel,
                                                  int* __restrict__ selg, float* __restrict__ gsel,
                                                  const u16* __restrict__ wd, char* __restrict__ wdt) {
  __shared__ __attribute__((aligned(1024))) char smem[K4_CAND + 256];
  const int tid = threadIdx.x, lane = tid & 63, q = lane >> 4, r = lane & 15;
  const int w = __builtin_amdgcn_readfirstlane(tid >> 6);
  if (blockIdx.x >= 96) {
    if (w == 8) return;
    const int b2 = blockIdx.x - 96;
    const char* srcp = (const char*)wd + (size_t)b2 * 16 * 1280;
    u32x4 v[3];
#pragma unroll
    for (int i = 0; i < 3; ++i) { const int c = i * 512 + tid; if (c < 1280) v[i] = __builtin_nontemporal_load((const u32x4*)(srcp + c * 16)); }
#pragma unroll
    for (int i = 0; i < 3; ++i) { const int c = i * 512 + tid; if (c < 1280) *(u32x4*)(smem + (c / 80) * 1296 + (c % 80) * 16) = v[i]; }
    WAIT_L(0);
    __builtin_amdgcn_s_barrier();
#pragma unroll
    for (int i = 0; i < 3; ++i) {
      const int d = i * 512 + tid;
      if (d < 1280) {
        const int cg = d >> 5, rw = (d & 31) >> 1, hf = d & 1;
        const u32x4 o = *(const u32x4*)(smem + rw * 1296 + cg * 32 + hf * 16);
        *(u32x4*)(wdt + (size_t)cg * 49152 + (b2 * 16 + rw) * 32 + hf * 16) = o;
      }
    }
    return;
  }
  const int xcd = blockIdx.x & 7, jj = blockIdx.x >> 3;
  const int f0 = ((jj >> 2) * 32 + xcd * 4 + (jj & 3)) * 16;
  if (w == 8) {
#pragma unroll 4
    for (int p = 0; p < 20; ++p) {
      const int row = p * 32 + (lane >> 1);
      GLDS16(wg + (size_t)row * FF + f0 + (lane & 1) * 8, smem + p * 1024);
      GLDS16(wu + (size_t)row * FF + f0 + (lane & 1) * 8, smem + 20480 + p * 1024);
    }
    __builtin_amdgcn_s_barrier();
    WAIT_V(0);
    return;
  }
  unsigned key[4];
  {
    const u32x4 v = *(const u32x4*)(rsc + tid * 4);
#pragma unroll
    for (int i = 0; i < 4; ++i) {
      const unsigned u = v[i];
      const unsigned o = (u & 0x80000000u) ? ~u : (u | 0x80000000u);
      key[i] = (o & 0xffff0000u) | (0xffffu - (unsigned)(tid * 4 + i));
    }
  }
  unsigned* cand = (unsigned*)(smem + K4_CAND);
#pragma unroll
  for (int rnd = 0; rnd < 8; ++rnd) {
    const unsigned lm = max(max(key[0], key[1]), max(key[2], key[3]));
    const unsigned wm = wave_max_u32(lm);
#pragma unroll
    for (int i = 0; i < 4; ++i) key[i] = (key[i] == wm) ? 0u : key[i];
    if (lane == 0) cand[w * 8 + rnd] = wm;
  }
  WAIT_L(0);
  __builtin_amdgcn_s_barrier();
  unsigned myk = 0;
  {
    unsigned k2 = cand[lane];
#pragma unroll
    for (int rnd = 0; rnd < 8; ++rnd) {
      const unsigned wm = wave_max_u32(k2);
      k2 = (k2 == wm) ? 0u : k2;
      myk = (w == rnd) ? wm : myk;
    }
  }
  {
    const int row = (int)(0xffffu - (myk & 0xffffu));
    const float2 st = stats[row];
    const unsigned* xp = (const unsigned*)(x + (size_t)row * DM);
    const unsigned* yr = (const unsigned*)(ypart + (size_t)row * DM);
    const unsigned* yi = (const unsigned*)(ypart + (size_t)SEQ * DM + (size_t)row * DM);
    unsigned xv[5], rv[5], iv[5];
#pragma unroll
    for (int i = 0; i < 5; ++i) { xv[i] = xp[i * 64 + lane]; rv[i] = yr[i * 64 + lane]; iv[i] = yi[i * 64 + lane]; }
#pragma unroll
    for (int i = 0; i < 5; ++i) {
      const float h0 = rbf(lo2f(xv[i]) + rbf(lo2f(rv[i]) - lo2f(iv[i])));
      const float h1 = rbf(hi2f(xv[i]) + rbf(hi2f(rv[i]) - hi2f(iv[i])));
      const unsigned hn = pk2(rbf(h0 - st.x) * st.y, rbf(h1 - st.x) * st.y);
      *(unsigned*)(smem + K4_XS + w * XS_LD + (i * 64 + lane) * 4) = hn;
      if (blockIdx.x == 0) {
        hsel[w * 320 + i * 64 + lane] = pk2(h0, h1);
        hsel[2560 + w * 320 + i * 64 + lane] = hn;
      }
    }
    if (blockIdx.x == 0 && lane == 0) {
      const unsigned ob = myk & 0xffff0000u;
      const float rr = __uint_as_float((ob & 0x80000000u) ? (ob & 0x7fffffffu) : (~ob & 0xffff0000u));
      selg[w] = row;
      gsel[w] = rbf(1.0f / (1.0f + __expf(-rr)));
    }
  }
  WAIT_L(0);
  __builtin_amdgcn_s_barrier();
  float* red = (float*)(smem + K4_RED);
  {
    const char* tile = smem + (w >> 2) * 20480;
    f32x4 acc = {0.f, 0.f, 0.f, 0.f};
#pragma unroll
    for (int ks = 0; ks < 5; ++ks) {
      const int k0 = (w & 3) * 160 + ks * 32;
      bf16x8 af = *(const bf16x8*)(smem + K4_XS + (r & 7) * XS_LD + (k0 + q * 8) * 2);
      if (r >= 8) af = (bf16x8){0, 0, 0, 0, 0, 0, 0, 0};
      const bf16x8 bw = ldtr_frag(tile, k0, lane);
      acc = __builtin_amdgcn_mfma_f32_16x16x32_bf16(af, bw, acc, 0, 0, 0);
    }
    if (lane < 32) *(f32x4*)(red + w * 128 + lane * 4) = acc;
  }
  WAIT_L(0);
  __builtin_amdgcn_s_barrier();
  if (tid < 64) {
    const int tk = tid >> 3, pr = tid & 7;
    float av[2];
#pragma unroll
    for (int e = 0; e < 2; ++e) {
      const int li = ((tk >> 2) * 16 + pr * 2 + e) * 4 + (tk & 3);
      float g = 0.f, u = 0.f;
#pragma unroll
      for (int ww = 0; ww < 4; ++ww) {
        g += red[ww * 128 + li];
        u += red[(ww + 4) * 128 + li];
      }
      g = rbf(g);
      u = rbf(u);
      const float sg = rbf(g / (1.0f + __expf(-g)));
      av[e] = sg * u;
    }
    act[tk * 768 + (f0 >> 1) + pr] = pk2(av[0], av[1]);
  }
}

#define AC_LD 3088
#define K5_AS 49152
#define K5_RED (49152 + 8 * AC_LD)
__global__ __launch_bounds__(576) void k5_down(const char* __restrict__ wdt, const u32x4* __restrict__ act,
                                                const unsigned* __restrict__ hsel, const int* __restrict__ selg,
                                                const float* __restrict__ gsel, u16* __restrict__ out) {
  __shared__ __attribute__((aligned(1024))) char smem[K5_RED + 4096];
  const int tid = threadIdx.x, lane = tid & 63, q = lane >> 4, r = lane & 15;
  const int w = __builtin_amdgcn_readfirstlane(tid >> 6);
  const int xcd = blockIdx.x & 7, jj = blockIdx.x >> 3;
  const int c0 = (xcd * 5 + jj) * 16;
  if (w == 8) {
#pragma unroll 4
    for (int p = 0; p < 48; ++p) GLDS16(wdt + (size_t)(c0 >> 4) * 49152 + p * 1024 + lane * 16, smem + p * 1024);
    WAIT_V(0);
    return;
  }
  u32x4 av[3];
#pragma unroll
  for (int i = 0; i < 3; ++i) av[i] = act[i * 512 + tid];
  const int tk = (tid >> 3) & 7, pr = tid & 7;
  const unsigned hv = hsel[tk * 320 + (c0 >> 1) + pr];
  const unsigned nv = hsel[2560 + tk * 320 + (c0 >> 1) + pr];
  const int orow = selg[tk];
  const float sgm = gsel[tk];
#pragma unroll
  for (int i = 0; i < 3; ++i) {
    const int idx = i * 512 + tid;
    *(u32x4*)(smem + K5_AS + (idx / 192) * AC_LD + (idx % 192) * 16) = av[i];
  }
  WAIT_L(0);
  __builtin_amdgcn_s_barrier();
  float* red = (float*)(smem + K5_RED);
  {
    f32x4 acc = {0.f, 0.f, 0.f, 0.f};
#pragma unroll
    for (int ks = 0; ks < 6; ++ks) {
      const int k0 = w * 192 + ks * 32;
      bf16x8 af = *(const bf16x8*)(smem + K5_AS + (r & 7) * AC_LD + (k0 + q * 8) * 2);
      if (r >= 8) af = (bf16x8){0, 0, 0, 0, 0, 0, 0, 0};
      const bf16x8 bw = ldtr_frag(smem, k0, lane);
      acc = __builtin_amdgcn_mfma_f32_16x16x32_bf16(af, bw, acc, 0, 0, 0);
    }
    if (lane < 32) *(f32x4*)(red + w * 128 + lane * 4) = acc;
  }
  WAIT_L(0);
  __builtin_amdgcn_s_barrier();
  if (tid < 64) {
    float ov[2];
#pragma unroll
    for (int e = 0; e < 2; ++e) {
      const int li = ((tk >> 2) * 16 + pr * 2 + e) * 4 + (tk & 3);
      float y = 0.f;
#pragma unroll
      for (int ww = 0; ww < 8; ++ww) y += red[ww * 128 + li];
      y = rbf(rbf(y) * sgm);
      const float h = e ? hi2f(hv) : lo2f(hv), hn = e ? hi2f(nv) : lo2f(nv);
      ov[e] = h + rbf(hn + y);
    }
    *(unsigned*)(out + (size_t)orow * DM + c0 + pr * 2) = pk2(ov[0], ov[1]);
  }
}

extern "C" void kernel_launch(void* const* d_in, const int* in_sizes, int n_in, void* d_out, int out_size,
                              void* d_ws, size_t ws_size, hipStream_t stream) {
  const u16* x = (const u16*)d_in[0];
  const u16* wsre = (const u16*)d_in[1];
  const u16* wsim = (const u16*)d_in[2];
  const u16* wdre = (const u16*)d_in[3];
  const u16* wdim = (const u16*)d_in[4];
  const u16* wr = (const u16*)d_in[6];
  const u16* wg = (const u16*)d_in[7];
  const u16* wu = (const u16*)d_in[8];
  const u16* wdn = (const u16*)d_in[9];
  u16* out = (u16*)d_out;
  char* ws = (char*)d_ws;
  u16* abT = (u16*)ws;
  u16* ypart = (u16*)(ws + 5242880);
  float* rsc = (float*)(ws + 10485760);
  float2* stats = (float2*)(ws + 10485760 + 8192);
  unsigned* act = (unsigned*)(ws + 10485760 + 8192 + 16384);
  unsigned* hsel = (unsigned*)(ws + 10485760 + 8192 + 16384 + 24576);
  int* selg = (int*)(ws + 10485760 + 8192 + 16384 + 24576 + 20480);
  float* gsel = (float*)(ws + 10485760 + 8192 + 16384 + 24576 + 20480 + 64);

  k1_ln_gemm<<<256, 512, 0, stream>>>(x, wdre, wdim, abT, wsre, wsim);
  k2_gemm<<<256, 512, 0, stream>>>(wsre, wsim, abT, ypart);
  k3_ln2<<<256, 576, 0, stream>>>(x, ypart, wr, out, rsc, stats, wg, wu, wdn);
  char* wdt = ws + (12u << 20);
  k4_gateup<<<192, 576, 0, stream>>>(x, ypart, rsc, stats, wg, wu, act, hsel, selg, gsel, wdn, wdt);
  k5_down<<<40, 576, 0, stream>>>(wdt, (const u32x4*)act, hsel, selg, gsel, out);
}
```
